```python
import torch

def _softmax(x, axis=-1):
    return torch.softmax(x, dim=axis)
def _gelu(x):
    return torch.nn.functional.gelu(x, approximate="tanh")
def _silu(x):
    return torch.nn.functional.silu(x)
def _sigmoid(x):
    return torch.sigmoid(x)
def _layernorm(x, axis=-1, eps=1e-5):
    m = x.mean(dim=axis, keepdim=True)
    v = x.var(dim=axis, keepdim=True, unbiased=False)
    return (x - m) / torch.sqrt(v + eps)
def _scatter_add(tgt, idx, src, axis=0):
    return tgt.clone().index_add_(axis, idx, src)
def _segment_sum(src, seg, n):
    out = torch.zeros((n,) + src.shape[1:], dtype=src.dtype)
    return out.index_add_(0, seg, src)
def _pad(x, pw):
    flat = tuple(v for pair in reversed(pw) for v in pair)
    return torch.nn.functional.pad(x, flat)

kernel_name = 'concat_heads__ssm_conv_h3__ssm_conv_h3__prompt'

def setup_inputs():

    _w = (lambda a: (lambda t: t.to(torch.bfloat16) if t.is_floating_point() else t)(torch.as_tensor(a)))
    def _A_setup():
        B, S, D = 1, 2048, 640
        ks = 4
        return {
            "q": _w((lambda *s: torch.randn(*s, dtype=torch.bfloat16) * 0.05)(B, S, D)),
            "k": _w((lambda *s: torch.randn(*s, dtype=torch.bfloat16) * 0.05)(B, S, D)),
            "v": _w((lambda *s: torch.randn(*s, dtype=torch.bfloat16) * 0.05)(B, S, D)),
            "shift_k": _w((lambda *s: torch.randn(*s, dtype=torch.bfloat16) * 0.05)(D, ks) * 0.5),
            "ssm_k": _w((lambda *s: torch.randn(*s, dtype=torch.bfloat16) * 0.05)(D, S) * (1.0 / S ** 0.5)),
            "ssm_D": _w((lambda *s: torch.randn(*s, dtype=torch.bfloat16) * 0.05)(D)),
        }
    _A_in = _A_setup()

    def _B_setup():
        B, S, D = 1, 2048, 640
        ks = 4
        return {
            "q": _w((lambda *s: torch.randn(*s, dtype=torch.bfloat16) * 0.05)(B, S, D)),
            "k": _w((lambda *s: torch.randn(*s, dtype=torch.bfloat16) * 0.05)(B, S, D)),
            "v": _w((lambda *s: torch.randn(*s, dtype=torch.bfloat16) * 0.05)(B, S, D)),
            "shift_k": _w((lambda *s: torch.randn(*s, dtype=torch.bfloat16) * 0.05)(D, ks) * 0.5),
            "ssm_k": _w((lambda *s: torch.randn(*s, dtype=torch.bfloat16) * 0.05)(D, S) * (1.0 / S ** 0.5)),
            "ssm_D": _w((lambda *s: torch.randn(*s, dtype=torch.bfloat16) * 0.05)(D)),
        }
    _B_in = _B_setup()

    return {
        "A_q": _A_in["q"],
        "A_k": _A_in["k"],
        "A_v": _A_in["v"],
        "A_shift_k": _A_in["shift_k"],
        "A_ssm_k": _A_in["ssm_k"],
        "A_ssm_D": _A_in["ssm_D"],
        "B_q": _B_in["q"],
        "B_k": _B_in["k"],
        "B_v": _B_in["v"],
        "B_shift_k": _B_in["shift_k"],
        "B_ssm_k": _B_in["ssm_k"],
        "B_ssm_D": _B_in["ssm_D"],
    }

def reference(A_q, A_k, A_v, A_shift_k, A_ssm_k, A_ssm_D, B_q, B_k, B_v, B_shift_k, B_ssm_k, B_ssm_D):
    def _A_ref(q, k, v, shift_k, ssm_k, ssm_D):
        B, S, D = q.shape
        ks = shift_k.shape[-1]
        k_pad = _pad(k, ((0, 0), (ks - 1, 0), (0, 0)))
        k_shift = torch.zeros_like(k)
        for t in range(ks):
            k_shift = k_shift + k_pad[:, t:t + S, :] * shift_k[:, ks - 1 - t][None, None, :]
        kv = k_shift * v
        L = 2 * S
        kv_pad = _pad(kv, ((0, 0), (0, S), (0, 0)))
        kf_pad = _pad(ssm_k, ((0, 0), (0, S)))
        kv_f = (lambda x, axes=(-2,-1): torch.fft.rfft2(x.float(), dim=axes))(kv_pad[:, None, :, :], axes=(1, 2))
        kf_f = (lambda x, axes=(-2,-1): torch.fft.rfft2(x.float(), dim=axes))(kf_pad[:, None, :], axes=(1, 2))
        kf_f = (lambda x, *d: x.permute(*d))(kf_f, 1, 2, 0)
        y_f = kv_f * kf_f[None, :, :, :]
        y = (lambda x, s, axes=(-2,-1): torch.fft.irfft2(x, s=s, dim=axes).to(torch.bfloat16))(y_f, (1, L), axes=(1, 2))[:, 0, :S, :]
        y = y + kv * ssm_D[None, None, :]
        return q * y

    def _B_ref(q, k, v, shift_k, ssm_k, ssm_D):
        B, S, D = q.shape
        ks = shift_k.shape[-1]
        k_pad = _pad(k, ((0, 0), (ks - 1, 0), (0, 0)))
        k_shift = torch.zeros_like(k)
        for t in range(ks):
            k_shift = k_shift + k_pad[:, t:t + S, :] * shift_k[:, ks - 1 - t][None, None, :]
        kv = k_shift * v
        L = 2 * S
        kv_pad = _pad(kv, ((0, 0), (0, S), (0, 0)))
        kf_pad = _pad(ssm_k, ((0, 0), (0, S)))
        kv_f = (lambda x, axes=(-2,-1): torch.fft.rfft2(x.float(), dim=axes))(kv_pad[:, None, :, :], axes=(1, 2))
        kf_f = (lambda x, axes=(-2,-1): torch.fft.rfft2(x.float(), dim=axes))(kf_pad[:, None, :], axes=(1, 2))
        kf_f = (lambda x, *d: x.permute(*d))(kf_f, 1, 2, 0)
        y_f = kv_f * kf_f[None, :, :, :]
        y = (lambda x, s, axes=(-2,-1): torch.fft.irfft2(x, s=s, dim=axes).to(torch.bfloat16))(y_f, (1, L), axes=(1, 2))[:, 0, :S, :]
        y = y + kv * ssm_D[None, None, :]
        return q * y

    a = _A_ref(A_q, A_k, A_v, A_shift_k, A_ssm_k, A_ssm_D)
    b = _B_ref(B_q, B_k, B_v, B_shift_k, B_ssm_k, B_ssm_D)
    return torch.concatenate([a, b], axis=-1)
```

```cpp
#include <hip/hip_runtime.h>
#include <hip/hip_bf16.h>
#include <stdint.h>

#define S 2048
#define D 640

static __device__ __forceinline__ float bf2f(uint16_t v) { return __uint_as_float(((uint32_t)v) << 16); }
static __device__ __forceinline__ uint16_t f2bf(float f) {
    __hip_bfloat16 b = __float2bfloat16(f);
    return *(uint16_t*)&b;
}
static __device__ __forceinline__ float rbf(float f) { return bf2f(f2bf(f)); }

#define EXACT 0

__global__ void kv_kernel(const uint16_t* k0, const uint16_t* v0, const uint16_t* w0,
                          const uint16_t* k1, const uint16_t* v1, const uint16_t* w1, uint16_t* xT) {
    int idx = blockIdx.x * blockDim.x + threadIdx.x;
    int c = idx % 1280, s = idx / 1280;
    const uint16_t *k, *v, *w; int d;
    if (c < D) { k = k0; v = v0; w = w0; d = c; } else { k = k1; v = v1; w = w1; d = c - D; }
    float acc = 0.f;
    for (int t = 0; t < 4; t++) {
        int r = s - 3 + t;
        float kk = r >= 0 ? bf2f(k[r * D + d]) : 0.f;
        float ww = bf2f(w[d * 4 + 3 - t]);
        acc = fmaf(kk, ww, acc);
    }
    xT[c * S + s] = f2bf(acc * bf2f(v[s * D + d]));
}

__global__ void conv_kernel(const uint16_t* xT, const uint16_t* q0, const uint16_t* h0, const uint16_t* D0,
                            const uint16_t* q1, const uint16_t* h1, const uint16_t* D1, uint16_t* out) {
    __shared__ float xs[S], hs[S];
    int c = blockIdx.x;
    const uint16_t *q, *h, *Dp; int d;
    if (c < D) { q = q0; h = h0; Dp = D0; d = c; } else { q = q1; h = h1; Dp = D1; d = c - D; }
    for (int i = threadIdx.x; i < S; i += blockDim.x) { xs[i] = bf2f(xT[c * S + i]); hs[i] = bf2f(h[d * S + i]); }
    __syncthreads();
    float dd = bf2f(Dp[d]);
    for (int s = threadIdx.x; s < S; s += blockDim.x) {
        float acc = 0.f;
        for (int j = 0; j <= s; j++) acc = fmaf(xs[j], hs[s - j], acc);
        float y = acc + xs[s] * dd;
        out[s * 1280 + c] = f2bf(bf2f(q[s * D + d]) * y);
    }
}

extern "C" void kernel_launch(void* const* d_in, const int* in_sizes, int n_in,
                              void* d_out, int out_size, void* d_ws, size_t ws_size,
                              hipStream_t stream) {
    const uint16_t* const* in = (const uint16_t* const*)d_in;
    uint16_t* xT = (uint16_t*)d_ws;
    kv_kernel<<<S * 1280 / 256, 256, 0, stream>>>(in[1], in[2], in[3], in[7], in[8], in[9], xT);
    conv_kernel<<<1280, 256, 0, stream>>>(xT, in[0], in[4], in[5], in[6], in[10], in[11], (uint16_t*)d_out);
}
```

```cpp
#include <hip/hip_runtime.h>
#include <stdint.h>

typedef uint16_t u16;
typedef uint32_t u32;
typedef u32 u32x2 __attribute__((ext_vector_type(2)));
typedef u32 u32x4 __attribute__((ext_vector_type(4)));
typedef float f32x2 __attribute__((ext_vector_type(2)));
typedef float f32x4 __attribute__((ext_vector_type(4)));
typedef __bf16 bf16x2 __attribute__((ext_vector_type(2)));
typedef __bf16 bf16x8 __attribute__((ext_vector_type(8)));
#define LAS __attribute__((address_space(3)))

#define EXACT 0

constexpr int S = 2048, D = 640;

template <int L, int C> struct Geo {
    static constexpr int H_COPY = (L / 32 + 8) * 64;
    static constexpr int H_CH = 2 * H_COPY;
    static constexpr int X_BOFF = 2 * L + 128;
    static constexpr int X_CH = X_BOFF + 2 * L + 64 + (C == 8 ? 16 : 8);
    static constexpr int LDS_X = C * H_CH;
    static constexpr int END = LDS_X + C * X_CH;
};
typedef Geo<2048, 8> GH;
typedef Geo<1024, 16> GL;
constexpr int LDS_CNT = (GH::END > GL::END ? GH::END : GL::END);
constexpr int LDS_TOTAL = LDS_CNT + 64;

struct Params {
    const u16* q[2]; const u16* k[2]; const u16* v[2]; const u16* w[2]; const u16* h[2]; const u16* dd[2];
    u16* out;
};

static __device__ __forceinline__ u32 pack2(float lo, float hi) {
    f32x2 f = {lo, hi};
    bf16x2 b = __builtin_convertvector(f, bf16x2);
    return __builtin_bit_cast(u32, b);
}
static __device__ __forceinline__ float rbf(float a) { return __builtin_bit_cast(float, pack2(0.f, a)); }
static __device__ __forceinline__ f32x2 rbf2(f32x2 a) { f32x2 r = {rbf(a.x), rbf(a.y)}; return r; }
static __device__ __forceinline__ f32x2 unpack2(u32 d) {
    f32x2 r = {__builtin_bit_cast(float, d << 16), __builtin_bit_cast(float, d & 0xffff0000u)};
    return r;
}
static __device__ __forceinline__ void wait_cnt(LAS u32* c, u32 n) {
    u32 tv, ts, tc;
    asm volatile(
        "s_mov_b32 %2, 0x400000\n"
        "W%=:\n\t"
        "ds_read_b32 %0, %3\n\t"
        "s_waitcnt lgkmcnt(0)\n\t"
        "v_readfirstlane_b32 %1, %0\n\t"
        "s_cmp_ge_u32 %1, %4\n\t"
        "s_cbranch_scc1 E%=\n\t"
        "s_sleep 1\n\t"
        "s_sub_u32 %2, %2, 1\n\t"
        "s_cbranch_scc0 W%=\n"
        "E%=:"
        : "=&v"(tv), "=&s"(ts), "=&s"(tc) : "v"(c), "s"(n) : "memory", "scc");
}
static __device__ __forceinline__ void signal_cnt(LAS u32* c, int lane) {
    u32 one = 1;
    unsigned long long sv;
    asm volatile("s_mov_b64 %0, exec\n\ts_mov_b64 exec, 1\n\tds_add_u32 %1, %2\n\ts_mov_b64 exec, %0"
                 : "=&s"(sv) : "v"(c), "v"(one) : "memory");
}
#define STAMP(i)
#define STDECL
#define STOUT()

typedef volatile LAS u32x2* vl64;
static __device__ __forceinline__ bf16x8 ldfrag(LAS char* a0, LAS char* a1, int off) {
    u32x2 lo = *(vl64)(a0 + off);
    u32x2 hi = *(vl64)(a1 + off);
    u32x4 r = {lo.x, lo.y, hi.x, hi.y};
    return __builtin_bit_cast(bf16x8, r);
}

struct KV { u32x2 k[4], v[4], halo[3]; };

template <typename RS>
static __device__ __forceinline__ void load_kv(KV& t, RS rk, RS rv, int voff, int hoff) {
#pragma unroll
    for (int r = 0; r < 4; r++) t.k[r] = __builtin_amdgcn_raw_buffer_load_b64(rk, voff + 1280 * r, 0, 0);
#pragma unroll
    for (int r = 0; r < 3; r++) t.halo[r] = __builtin_amdgcn_raw_buffer_load_b64(rk, hoff + 1280 * r, 0, 0);
#pragma unroll
    for (int r = 0; r < 4; r++) t.v[r] = __builtin_amdgcn_raw_buffer_load_b64(rv, voff + 1280 * r, 0, 0);
}

template <typename G, int SHR>
static __device__ __forceinline__ void convert_kv(const KV& t, const f32x2 (&W)[2][4], LAS char* xa) {
#pragma unroll
    for (int d = 0; d < 2; d++) {
        f32x2 kk[7];
#pragma unroll
        for (int r = 0; r < 3; r++)
            kk[r] = unpack2(__builtin_amdgcn_update_dpp(t.halo[r][d], t.k[r + 1][d], 0x110 + SHR, 0xf, 0xf, false));
#pragma unroll
        for (int r = 0; r < 4; r++) kk[3 + r] = unpack2(t.k[r][d]);
        f32x2 x[4];
#pragma unroll
        for (int r = 0; r < 4; r++) {
            f32x2 a = kk[r] * W[d][3];
            a = __builtin_elementwise_fma(kk[r + 1], W[d][2], a);
            a = __builtin_elementwise_fma(kk[r + 2], W[d][1], a);
            a = __builtin_elementwise_fma(kk[r + 3], W[d][0], a);
            x[r] = a * unpack2(t.v[r][d]);
        }
#pragma unroll
        for (int c = 0; c < 2; c++) {
            u32 d0 = pack2(x[3][c], x[2][c]), d1 = pack2(x[1][c], x[0][c]);
            LAS char* a = xa + (2 * d + c) * G::X_CH;
            u32x2 dd = {d0, d1};
            *(LAS u32x2*)a = dd;
            *(LAS u32*)(a + G::X_BOFF + 4) = d0;
            *(LAS u32*)(a + G::X_BOFF + 8) = d1;
        }
    }
}

#define MFMA(a, b, c) __builtin_amdgcn_mfma_f32_16x16x32_bf16(a, b, c, 0, 0, 0)
#define SB() __builtin_amdgcn_sched_barrier(0)
#define PIN() asm volatile("" : "+v"(acc[0]), "+v"(acc[1]), "+v"(acc[2]), "+v"(acc[3]))
struct Addr { LAS char* a[4][2]; LAS char* x; };
static __device__ __forceinline__ bf16x8 ldH(const Addr& A, int ph, int mu) {
    return (ph & 4) ? ldfrag(A.a[ph & 3][1], A.a[ph & 3][0], 256 + 512 * mu) : ldfrag(A.a[ph & 3][0], A.a[ph & 3][1], 512 * mu);
}
static __device__ __forceinline__ bf16x8 ldX(const Addr& A, int ph, int p) { return ldfrag(A.x, A.x + 32, 64 * ph + 512 * (7 - p)); }

template <int GI>
static __device__ __forceinline__ void load_xd(u32 (&xd)[4], LAS char* xdp) {
#pragma unroll
    for (int i = 0; i < 2; i++)
#pragma unroll
        for (int rp = 0; rp < 2; rp++) xd[2 * i + rp] = *(volatile LAS u32*)(xdp + 2 * (768 + 32 - 512 * GI - 256 * i - 32 * rp));
}
template <int GI>
static __device__ __forceinline__ void finish_group(const f32x4 (&acc)[4], const u32 (&xd)[4], LAS char* yp0, LAS char* yp1, float dd) {
#pragma unroll
    for (int i = 0; i < 2; i++) {
#pragma unroll
        for (int rp = 0; rp < 2; rp++) {
            f32x2 xf = unpack2(xd[2 * i + rp]);
            const f32x4& a = acc[2 * GI + i];
            float y0, y1;
            y0 = __builtin_fmaf(xf.y, dd, a[2 * rp]);
            y1 = __builtin_fmaf(xf.x, dd, a[2 * rp + 1]);
            *(LAS u32*)((rp ? yp1 : yp0) + 64 * (24 - 16 * GI) + 512 * i) = pack2(y0, y1);
        }
    }
}

template <typename G, int NR>
static __device__ __forceinline__ void put_h(const u32x4* hv, LAS char* hch, int lane) {
    u32 prev = 0;
#pragma unroll
    for (int r = 0; r < NR; r++) {
        u32 pl = __builtin_amdgcn_update_dpp(prev, hv[r].w, 0x138, 0xf, 0xf, false);
        prev = __builtin_amdgcn_readlane(hv[r].w, 63);
        u32x4 s;
        s.x = __builtin_amdgcn_alignbit(hv[r].x, pl, 16);
        s.y = __builtin_amdgcn_alignbit(hv[r].y, hv[r].x, 16);
        s.z = __builtin_amdgcn_alignbit(hv[r].z, hv[r].y, 16);
        s.w = __builtin_amdgcn_alignbit(hv[r].w, hv[r].z, 16);
        int u = 28 + 64 * r + lane;
        int o = 16 * (u ^ ((u >> 3) & 2));
        *(LAS u32x4*)(hch + o) = s;
        *(LAS u32x4*)(hch + G::H_COPY + (o ^ 16)) = hv[r];
    }
}
template <typename G, int L>
static __device__ __forceinline__ void zero_pads(LAS char* hch, LAS char* xch, int lane) {
    u32x4 z = {0, 0, 0, 0};
    if (lane < 56) *(LAS u32x4*)(hch + (lane >= 28 ? G::H_COPY - 448 : 0) + 16 * lane) = z;
    if (lane < 32) *(LAS u32*)(xch + 2 * L + (lane >= 16 ? G::X_BOFF - 60 : 0) + 4 * lane) = 0;
}

template <int LO>
static __device__ __forceinline__ void mem_waves(const Params& p, LAS char* smem, int br, int c0, int wave, int lane) {
    typedef Geo<LO ? 1024 : 2048, LO ? 16 : 8> G;
    constexpr int L = LO ? 1024 : 2048;
    LAS u32* cnt = (LAS u32*)(smem + LDS_CNT);
    STDECL;
    STAMP(0);
    {
        constexpr int SH = LO ? 2 : 1;
        constexpr int UNIT = L / 4;
        const int set = wave >> 2, wq = wave & 3;
        const int quad = lane >> SH, part = lane & ((1 << SH) - 1);
        auto rk = __builtin_amdgcn_make_buffer_rsrc((void*)p.k[br], 0, S * D * 2, 0x00020000);
        auto rv = __builtin_amdgcn_make_buffer_rsrc((void*)p.v[br], 0, S * D * 2, 0x00020000);
        auto rq = __builtin_amdgcn_make_buffer_rsrc((void*)p.q[br], 0, S * D * 2, 0x00020000);
        auto ro = __builtin_amdgcn_make_buffer_rsrc((void*)p.out, 0, S * D * 4, 0x00020000);
        const int rin = (UNIT / 4) * wq + 4 * quad;
        const int rowa = UNIT * set + rin;
        const int voff = rowa * 1280 + c0 * 2 + part * 8;
        const bool hl = (lane & 15) < (1 << SH);
        const u32x4* wp = (const u32x4*)(p.w[br] + (c0 + 4 * part) * 4);
        u32x4 w0 = wp[0], w1 = wp[1];
        if (wave == 0 && lane < 8) cnt[lane] = 0;
        asm volatile("s_waitcnt lgkmcnt(0)" ::: "memory");
        __builtin_amdgcn_s_barrier();
        if (set == 0) __builtin_amdgcn_s_setprio(3); else __builtin_amdgcn_s_setprio(2);
        KV t0, t1;
        load_kv(t0, rk, rv, voff, (hl && rowa > 0) ? voff - 3840 : (int)0x80000000);
        __builtin_amdgcn_s_setprio(3);
        f32x2 W[2][4];
        {
            f32x2 a0 = unpack2(w0.x), a1 = unpack2(w0.y), b0 = unpack2(w0.z), b1 = unpack2(w0.w);
            W[0][0] = f32x2{a0.x, b0.x}; W[0][1] = f32x2{a0.y, b0.y}; W[0][2] = f32x2{a1.x, b1.x}; W[0][3] = f32x2{a1.y, b1.y};
            a0 = unpack2(w1.x); a1 = unpack2(w1.y); b0 = unpack2(w1.z); b1 = unpack2(w1.w);
            W[1][0] = f32x2{a0.x, b0.x}; W[1][1] = f32x2{a0.y, b0.y}; W[1][2] = f32x2{a1.x, b1.x}; W[1][3] = f32x2{a1.y, b1.y};
        }
        LAS char* xa = smem + G::LDS_X + 4 * part * G::X_CH + 2 * (L - 4 - rowa);
        convert_kv<G, (1 << SH)>(t0, W, xa);
        signal_cnt(cnt + (LO ? 0 : set), lane);
        STAMP(1);
        __builtin_amdgcn_s_setprio(1);
        load_kv(t1, rk, rv, voff + 2 * UNIT * 1280, hl ? voff + 2 * UNIT * 1280 - 3840 : (int)0x80000000);
        __builtin_amdgcn_s_setprio(3);
        convert_kv<G, (1 << SH)>(t1, W, xa - 4 * UNIT);
        signal_cnt(cnt + (LO ? 1 : 2 + set), lane);
        STAMP(2);
        __builtin_amdgcn_s_setprio(0);
        constexpr int NE = LO ? 2 : 1;
        u32x2 qv[NE][4];
#pragma unroll
        for (int i = 0; i < NE; i++)
#pragma unroll
            for (int r = 0; r < 4; r++)
                qv[i][r] = __builtin_amdgcn_raw_buffer_load_b64(rq, voff + (LO ? i : 1) * 2 * UNIT * 1280 + 1280 * r, 0, 0);
        const int ooff = rowa * 2560 + br * 1280 + c0 * 2 + part * 8;
#pragma unroll
        for (int i = 0; i < NE; i++) {
            const int gy = LO ? i : set;
            const int ry = LO ? rowa : rin;
            wait_cnt(cnt + 4 + gy, LO ? 16 : 8);
            STAMP(3 + 2 * i);
            LAS char* yb = smem + 4 * part * G::H_CH + 64 * (24 - 16 * gy) + ((2 * ry) ^ (part << (8 - SH)));
            u32x2 y[4];
#pragma unroll
            for (int c = 0; c < 4; c++) y[c] = *(LAS u32x2*)(yb + c * G::H_CH);
#pragma unroll
            for (int r = 0; r < 4; r++) {
                u32x2 o;
#pragma unroll
                for (int d = 0; d < 2; d++) {
                    f32x2 qf = unpack2(qv[i][r][d]);
                    f32x2 ya = unpack2(y[2 * d][r >> 1]), yc = unpack2(y[2 * d + 1][r >> 1]);
                    o[d] = pack2(qf.x * ya[r & 1], qf.y * yc[r & 1]);
                }
                __builtin_amdgcn_raw_buffer_store_b64(o, ro, ooff + (LO ? i : 1) * 2 * UNIT * 2560 + 2560 * r, 0, 0);
            }
            STAMP(4 + 2 * i);
        }
    }
    STOUT();
}

template <bool lo>
static __device__ __forceinline__ void mfma_waves(const Params& p, LAS char* smem, int br, int c0, int wave, int lane) {
    LAS u32* cnt = (LAS u32*)(smem + LDS_CNT);
    STDECL;
    STAMP(0);
    const int cw = lo ? 2 * (wave - 8) : wave - 8;
    u32x4 hv[4];
    {
        const u32x4* hp = (const u32x4*)(p.h[br] + (size_t)(c0 + cw) * S);
        const int o2 = lo ? 256 : 128;
        hv[0] = hp[lane]; hv[1] = hp[64 + lane]; hv[2] = hp[o2 + lane]; hv[3] = hp[o2 + 64 + lane];
    }
    const u32 dw = *(const __attribute__((address_space(4))) u32*)(p.dd[br] + ((c0 + cw) & ~1));
    SB();
    __builtin_amdgcn_s_barrier();
    SB();
    const int h_ch = lo ? GL::H_CH : GH::H_CH, x_ch = lo ? GL::X_CH : GH::X_CH;
    LAS char* hch = smem + cw * h_ch;
    LAS char* xch = smem + (lo ? GL::LDS_X : GH::LDS_X) + cw * x_ch;
    if (lo) {
        zero_pads<GL, 1024>(hch, xch, lane);
        zero_pads<GL, 1024>(hch + GL::H_CH, xch + GL::X_CH, lane);
        put_h<GL, 2>(hv, hch, lane);
        put_h<GL, 2>(hv + 2, hch + GL::H_CH, lane);
    } else {
        zero_pads<GH, 2048>(hch, xch, lane);
        put_h<GH, 4>(hv, hch, lane);
    }
    STAMP(1);
    const int b = lane & 15, g = lane >> 4;
    Addr A;
    {
        LAS char* hb = hch + ((b & 1) ? (h_ch >> 1) : 0);
        const int ah = b >> 1, hsw = (8 * g) ^ ((b & 1) << 4);
#pragma unroll
        for (int i = 0; i < 4; i++) { int M = ah + i; int o = 64 * M + (hsw ^ ((M & 4) << 3)); A.a[i][0] = hb + o; A.a[i][1] = hb + (o ^ 32); }
        A.x = xch + ((b & 1) ? 0 : (lo ? GL::X_BOFF : GH::X_BOFF)) + 2 * (32 - 4 * ((b + 1) >> 1) + 4 * g);
    }
    const int ysw = (cw >> 2) << (lo ? 6 : 7);
    int yo = 128 * g + 4 * b;
    f32x4 acc[4];
#pragma unroll
    for (int t = 0; t < 4; t++) acc[t] = f32x4{0.f, 0.f, 0.f, 0.f};
    bf16x8 R[8], Q[8], T0, T1, T2, X0, X1, N0, N1;
    if (!lo) {
#pragma unroll
        for (int ph = 0; ph < 8; ph++) { R[ph] = ldH(A, ph, 3); Q[ph] = ldH(A, ph, 4); }
        T0 = ldH(A, 0, 5); T1 = ldH(A, 0, 6); T2 = ldH(A, 0, 7);
        wait_cnt(cnt + 0, 4); STAMP(2);
        X0 = ldX(A, 0, 0); X1 = ldX(A, 0, 1);
#pragma unroll
        for (int ph = 0; ph < 8; ph++) {
            SB();
            acc[3] = MFMA(T2, X0, acc[3]); SB();
            T2 = ph < 7 ? ldH(A, ph + 1, 7) : ldH(A, 0, 2); SB();
            acc[2] = MFMA(T1, X0, acc[2]); SB();
            if (ph < 7) N0 = ldX(A, ph + 1, 0);
            SB();
            acc[3] = MFMA(T1, X1, acc[3]); SB();
            T1 = ph < 7 ? ldH(A, ph + 1, 6) : ldH(A, 0, 1); SB();
            acc[1] = MFMA(T0, X0, acc[1]); SB();
            if (ph < 7) N1 = ldX(A, ph + 1, 1);
            SB();
            acc[2] = MFMA(T0, X1, acc[2]); SB();
            T0 = ldH(A, (ph + 1) & 7, 5); SB();
            acc[0] = MFMA(Q[ph], X0, acc[0]);
            acc[1] = MFMA(Q[ph], X1, acc[1]);
            acc[0] = MFMA(R[ph], X1, acc[0]);
            X0 = N0; X1 = N1; PIN();
        }
        SB(); STAMP(3);
        wait_cnt(cnt + 1, 4); STAMP(4);
        X0 = ldX(A, 0, 2); X1 = ldX(A, 0, 3);
#pragma unroll
        for (int ph = 0; ph < 8; ph++) {
            SB();
            acc[3] = MFMA(T0, X0, acc[3]); SB();
            T0 = ph < 7 ? ldH(A, ph + 1, 5) : ldH(A, 0, 0); SB();
            acc[0] = MFMA(T2, X0, acc[0]); SB();
            if (ph < 7) N0 = ldX(A, ph + 1, 2);
            SB();
            acc[1] = MFMA(T2, X1, acc[1]); SB();
            T2 = ldH(A, (ph + 1) & 7, 2); SB();
            acc[2] = MFMA(Q[ph], X0, acc[2]); SB();
            if (ph < 7) N1 = ldX(A, ph + 1, 3);
            SB();
            acc[3] = MFMA(Q[ph], X1, acc[3]); SB();
            Q[ph] = T1;
            if (ph < 7) T1 = ldH(A, ph + 1, 1);
            SB();
            acc[0] = MFMA(Q[ph], X1, acc[0]);
            acc[1] = MFMA(R[ph], X0, acc[1]);
            acc[2] = MFMA(R[ph], X1, acc[2]);
            X0 = N0; X1 = N1; PIN();
        }
        SB(); STAMP(5);
    }
    LAS u32* cx = cnt + (lo ? 0 : 2);
    const u32 need = lo ? 8 : 4;
    const int nc = lo ? 2 : 1;
#pragma unroll 1
    for (int c = 0; c < nc; c++) {
        if (lo) {
#pragma unroll
            for (int ph = 0; ph < 8; ph++) { R[ph] = ldH(A, ph, 3); Q[ph] = ldH(A, ph, 1); }
            T0 = ldH(A, 0, 0); T2 = ldH(A, 0, 2);
#pragma unroll
            for (int t = 0; t < 4; t++) acc[t] = f32x4{0.f, 0.f, 0.f, 0.f};
        }
        const float dd = __builtin_bit_cast(float, ((c0 + cw + c) & 1) ? (dw & 0xffff0000u) : (dw << 16));
        LAS char* xdp = xch + (2 * (256 - 32 - 2) - yo);
        LAS char* yp0 = hch + (yo ^ ysw);
        LAS char* yp1 = hch + ((yo + 64) ^ ysw);
        u32 xd[4];
        wait_cnt(cx, need); STAMP(6);
        X0 = ldX(A, 0, 4); X1 = ldX(A, 0, 5);
#pragma unroll
        for (int ph = 0; ph < 8; ph++) {
            SB();
            acc[2] = MFMA(T2, X0, acc[2]); SB();
            if (ph < 7) N0 = ldX(A, ph + 1, 4); else load_xd<0>(xd, xdp);
            SB();
            acc[3] = MFMA(T2, X1, acc[3]); SB();
            if (ph < 7) T2 = ldH(A, ph + 1, 2);
            SB();
            acc[3] = MFMA(R[ph], X0, acc[3]); SB();
            R[ph] = T0;
            if (ph < 7) T0 = ldH(A, ph + 1, 0);
            SB();
            acc[1] = MFMA(Q[ph], X0, acc[1]); SB();
            if (ph < 7) N1 = ldX(A, ph + 1, 5);
            SB();
            acc[0] = MFMA(R[ph], X0, acc[0]);
            acc[1] = MFMA(R[ph], X1, acc[1]);
            acc[2] = MFMA(Q[ph], X1, acc[2]);
            X0 = N0; X1 = N1; PIN();
        }
        SB();
        finish_group<0>(acc, xd, yp0, yp1, dd);
        signal_cnt(cnt + 4, lane); STAMP(7);
        wait_cnt(cx + 1, need); STAMP(8);
        X0 = ldX(A, 0, 6); X1 = ldX(A, 0, 7); N0 = ldX(A, 1, 6); N1 = ldX(A, 1, 7);
#pragma unroll
        for (int ph = 0; ph < 8; ph++) {
            SB();
            acc[2] = MFMA(R[ph], X0, acc[2]); SB();
            if (ph < 6) T0 = ldX(A, ph + 2, 6);
            if (ph == 6) load_xd<1>(xd, xdp);
            SB();
            acc[3] = MFMA(Q[ph], X0, acc[3]); SB();
            if (ph < 6) T1 = ldX(A, ph + 2, 7);
            SB();
            acc[3] = MFMA(R[ph], X1, acc[3]);
            X0 = N0; X1 = N1; N0 = T0; N1 = T1; PIN();
        }
        SB();
        finish_group<1>(acc, xd, yp0, yp1, dd);
        signal_cnt(cnt + 5, lane); STAMP(9);
        hch += h_ch; xch += x_ch;
#pragma unroll
        for (int i = 0; i < 4; i++) { A.a[i][0] += h_ch; A.a[i][1] += h_ch; }
        A.x += x_ch;
    }
    STOUT();
}

__global__ __launch_bounds__(1024) void h3_kernel(Params p) {
    extern __shared__ __attribute__((aligned(256))) char smem_raw[];
    LAS char* smem = (LAS char*)smem_raw;
    const int tid = threadIdx.x, lane = tid & 63;
    const int wave = __builtin_amdgcn_readfirstlane(tid >> 6);
    const int bid = blockIdx.x;
    const int x = bid & 7, j = bid >> 3;
    const int br = x >> 2, cb = (x & 3) * 160;
    const bool lo = j >= 20;
    const int c0 = cb + (lo ? 16 * (j - 20) : 8 * j);
    if (wave >= 8) { if (lo) mfma_waves<true>(p, smem, br, c0, wave, lane); else mfma_waves<false>(p, smem, br, c0, wave, lane); }
    else if (lo) mem_waves<1>(p, smem, br, c0, wave, lane);
    else mem_waves<0>(p, smem, br, c0, wave, lane);
}

extern "C" void kernel_launch(void* const* d_in, const int* in_sizes, int n_in,
                              void* d_out, int out_size, void* d_ws, size_t ws_size,
                              hipStream_t stream) {
    Params p;
    for (int i = 0; i < 2; i++) {
        p.q[i] = (const u16*)d_in[6 * i + 0];
        p.k[i] = (const u16*)d_in[6 * i + 1];
        p.v[i] = (const u16*)d_in[6 * i + 2];
        p.w[i] = (const u16*)d_in[6 * i + 3];
        p.h[i] = (const u16*)d_in[6 * i + 4];
        p.dd[i] = (const u16*)d_in[6 * i + 5];
    }
    p.out = (u16*)d_out;
    (void)hipFuncSetAttribute((const void*)h3_kernel, hipFuncAttributeMaxDynamicSharedMemorySize, LDS_TOTAL);
    h3_kernel<<<240, 1024, LDS_TOTAL, stream>>>(p);
}
```

```cpp
#include <hip/hip_runtime.h>
#include <stdint.h>

typedef uint16_t u16;
typedef uint32_t u32;
typedef u32 u32x2 __attribute__((ext_vector_type(2)));
typedef u32 u32x4 __attribute__((ext_vector_type(4)));
typedef float f32x2 __attribute__((ext_vector_type(2)));
typedef float f32x4 __attribute__((ext_vector_type(4)));
typedef __bf16 bf16x2 __attribute__((ext_vector_type(2)));
typedef __bf16 bf16x8 __attribute__((ext_vector_type(8)));
#define LAS __attribute__((address_space(3)))

#define LAUX 0
#define EXACT 0

constexpr int S = 2048, D = 640;

template <int L, int C> struct Geo {
    static constexpr int H_COPY = (L / 32 + 8) * 64;
    static constexpr int H_CH = 2 * H_COPY;
    static constexpr int X_BOFF = 2 * L + 128;
    static constexpr int X_CH = X_BOFF + 2 * L + 64 + (C == 8 ? 16 : 8);
    static constexpr int LDS_X = C * H_CH;
    static constexpr int END = LDS_X + C * X_CH;
};
typedef Geo<2048, 8> GH;
typedef Geo<1024, 16> GL;
constexpr int LDS_CNT = (GH::END > GL::END ? GH::END : GL::END);
constexpr int LDS_TOTAL = LDS_CNT + 64;

struct Params {
    const u16* q[2]; const u16* k[2]; const u16* v[2]; const u16* w[2]; const u16* h[2]; const u16* dd[2];
    u16* out;
};

static __device__ __forceinline__ u32 pack2(float lo, float hi) {
    f32x2 f = {lo, hi};
    bf16x2 b = __builtin_convertvector(f, bf16x2);
    return __builtin_bit_cast(u32, b);
}
static __device__ __forceinline__ float rbf(float a) { return __builtin_bit_cast(float, pack2(0.f, a)); }
static __device__ __forceinline__ f32x2 rbf2(f32x2 a) { f32x2 r = {rbf(a.x), rbf(a.y)}; return r; }
static __device__ __forceinline__ f32x2 unpack2(u32 d) {
    f32x2 r = {__builtin_bit_cast(float, d << 16), __builtin_bit_cast(float, d & 0xffff0000u)};
    return r;
}
static __device__ __forceinline__ void wait_cnt(LAS u32* c, u32 n) {
    u32 tv, ts, tc;
    asm volatile(
        "s_mov_b32 %2, 0x400000\n"
        "W%=:\n\t"
        "ds_read_b32 %0, %3\n\t"
        "s_waitcnt lgkmcnt(0)\n\t"
        "v_readfirstlane_b32 %1, %0\n\t"
        "s_cmp_ge_u32 %1, %4\n\t"
        "s_cbranch_scc1 E%=\n\t"
        "s_sleep 1\n\t"
        "s_sub_u32 %2, %2, 1\n\t"
        "s_cbranch_scc0 W%=\n"
        "E%=:"
        : "=&v"(tv), "=&s"(ts), "=&s"(tc) : "v"(c), "s"(n) : "memory", "scc");
}
static __device__ __forceinline__ void signal_cnt(LAS u32* c, int lane) {
    u32 one = 1;
    unsigned long long sv;
    asm volatile("s_mov_b64 %0, exec\n\ts_mov_b64 exec, 1\n\tds_add_u32 %1, %2\n\ts_mov_b64 exec, %0"
                 : "=&s"(sv) : "v"(c), "v"(one) : "memory");
}
#define STAMP(i)
#define STDECL
#define STOUT()

typedef volatile LAS u32x2* vl64;
static __device__ __forceinline__ bf16x8 ldfrag(LAS char* a0, LAS char* a1, int off) {
    u32x2 lo = *(vl64)(a0 + off);
    u32x2 hi = *(vl64)(a1 + off);
    u32x4 r = {lo.x, lo.y, hi.x, hi.y};
    return __builtin_bit_cast(bf16x8, r);
}

struct KV { u32x2 k[4], v[4], halo[3]; };

template <typename RS>
static __device__ __forceinline__ void load_kv(KV& t, RS rk, RS rv, int voff, int hoff) {
#pragma unroll
    for (int r = 0; r < 4; r++) t.k[r] = __builtin_amdgcn_raw_buffer_load_b64(rk, voff + 1280 * r, 0, LAUX);
#pragma unroll
    for (int r = 0; r < 3; r++) t.halo[r] = __builtin_amdgcn_raw_buffer_load_b64(rk, hoff + 1280 * r, 0, LAUX);
#pragma unroll
    for (int r = 0; r < 4; r++) t.v[r] = __builtin_amdgcn_raw_buffer_load_b64(rv, voff + 1280 * r, 0, LAUX);
}

template <typename G, int SHR>
static __device__ __forceinline__ void convert_kv(const KV& t, const f32x2 (&W)[2][4], LAS char* xa) {
#pragma unroll
    for (int d = 0; d < 2; d++) {
        f32x2 kk[7];
#pragma unroll
        for (int r = 0; r < 3; r++)
            kk[r] = unpack2(__builtin_amdgcn_update_dpp(t.halo[r][d], t.k[r + 1][d], 0x110 + SHR, 0xf, 0xf, false));
#pragma unroll
        for (int r = 0; r < 4; r++) kk[3 + r] = unpack2(t.k[r][d]);
        f32x2 x[4];
#pragma unroll
        for (int r = 0; r < 4; r++) {
            f32x2 a = kk[r] * W[d][3];
            a = __builtin_elementwise_fma(kk[r + 1], W[d][2], a);
            a = __builtin_elementwise_fma(kk[r + 2], W[d][1], a);
            a = __builtin_elementwise_fma(kk[r + 3], W[d][0], a);
            x[r] = a * unpack2(t.v[r][d]);
        }
#pragma unroll
        for (int c = 0; c < 2; c++) {
            u32 d0 = pack2(x[3][c], x[2][c]), d1 = pack2(x[1][c], x[0][c]);
            LAS char* a = xa + (2 * d + c) * G::X_CH;
            u32x2 dd = {d0, d1};
            *(LAS u32x2*)a = dd;
            *(LAS u32*)(a + G::X_BOFF + 4) = d0;
            *(LAS u32*)(a + G::X_BOFF + 8) = d1;
        }
    }
}

#define MFMA(a, b, c) __builtin_amdgcn_mfma_f32_16x16x32_bf16(a, b, c, 0, 0, 0)
#define SB() __builtin_amdgcn_sched_barrier(0)
#define PIN() asm volatile("" : "+v"(acc[0]), "+v"(acc[1]), "+v"(acc[2]), "+v"(acc[3]))
struct Addr { LAS char* a[4][2]; LAS char* x; };
static __device__ __forceinline__ bf16x8 ldH(const Addr& A, int ph, int mu) {
    return (ph & 4) ? ldfrag(A.a[ph & 3][1], A.a[ph & 3][0], 256 + 512 * mu) : ldfrag(A.a[ph & 3][0], A.a[ph & 3][1], 512 * mu);
}
static __device__ __forceinline__ bf16x8 ldX(const Addr& A, int ph, int p) { return ldfrag(A.x, A.x + 32, 64 * ph + 512 * (7 - p)); }

template <int GI>
static __device__ __forceinline__ void load_xd(u32 (&xd)[4], LAS char* xdp) {
#pragma unroll
    for (int i = 0; i < 2; i++)
#pragma unroll
        for (int rp = 0; rp < 2; rp++) xd[2 * i + rp] = *(volatile LAS u32*)(xdp + 2 * (768 + 32 - 512 * GI - 256 * i - 32 * rp));
}
template <int GI>
static __device__ __forceinline__ void finish_group(const f32x4 (&acc)[4], const u32 (&xd)[4], LAS char* yp0, LAS char* yp1, float dd) {
#pragma unroll
    for (int i = 0; i < 2; i++) {
#pragma unroll
        for (int rp = 0; rp < 2; rp++) {
            f32x2 xf = unpack2(xd[2 * i + rp]);
            const f32x4& a = acc[2 * GI + i];
            float y0, y1;
            y0 = __builtin_fmaf(xf.y, dd, a[2 * rp]);
            y1 = __builtin_fmaf(xf.x, dd, a[2 * rp + 1]);
            *(LAS u32*)((rp ? yp1 : yp0) + 64 * (24 - 16 * GI) + 512 * i) = pack2(y0, y1);
        }
    }
}

template <typename G, int NR>
static __device__ __forceinline__ void put_h(const u32x4* hv, LAS char* hch, int lane) {
    u32 prev = 0;
#pragma unroll
    for (int r = 0; r < NR; r++) {
        u32 pl = __builtin_amdgcn_update_dpp(prev, hv[r].w, 0x138, 0xf, 0xf, false);
        prev = __builtin_amdgcn_readlane(hv[r].w, 63);
        u32x4 s;
        s.x = __builtin_amdgcn_alignbit(hv[r].x, pl, 16);
        s.y = __builtin_amdgcn_alignbit(hv[r].y, hv[r].x, 16);
        s.z = __builtin_amdgcn_alignbit(hv[r].z, hv[r].y, 16);
        s.w = __builtin_amdgcn_alignbit(hv[r].w, hv[r].z, 16);
        int u = 28 + 64 * r + lane;
        int o = 16 * (u ^ ((u >> 3) & 2));
        *(LAS u32x4*)(hch + o) = s;
        *(LAS u32x4*)(hch + G::H_COPY + (o ^ 16)) = hv[r];
    }
}
template <typename G, int L>
static __device__ __forceinline__ void zero_pads(LAS char* hch, LAS char* xch, int lane) {
    u32x4 z = {0, 0, 0, 0};
    if (lane < 56) *(LAS u32x4*)(hch + (lane >= 28 ? G::H_COPY - 448 : 0) + 16 * lane) = z;
    if (lane < 32) *(LAS u32*)(xch + 2 * L + (lane >= 16 ? G::X_BOFF - 60 : 0) + 4 * lane) = 0;
}

template <int LO>
static __device__ __forceinline__ void mem_waves(const Params& p, LAS char* smem, int br, int c0, int wave, int lane) {
    typedef Geo<LO ? 1024 : 2048, LO ? 16 : 8> G;
    constexpr int L = LO ? 1024 : 2048;
    LAS u32* cnt = (LAS u32*)(smem + LDS_CNT);
    STDECL;
    STAMP(0);
    {
        constexpr int SH = LO ? 2 : 1;
        constexpr int UNIT = L / 4;
        const int set = wave >> 2, wq = wave & 3;
        const int quad = lane >> SH, part = lane & ((1 << SH) - 1);
        auto rk = __builtin_amdgcn_make_buffer_rsrc((void*)p.k[br], 0, S * D * 2, 0x00020000);
        auto rv = __builtin_amdgcn_make_buffer_rsrc((void*)p.v[br], 0, S * D * 2, 0x00020000);
        auto rq = __builtin_amdgcn_make_buffer_rsrc((void*)p.q[br], 0, S * D * 2, 0x00020000);
        auto ro = __builtin_amdgcn_make_buffer_rsrc((void*)p.out, 0, S * D * 4, 0x00020000);
        const int rin = (UNIT / 4) * wq + 4 * quad;
        const int rowa = UNIT * set + rin;
        const int voff = rowa * 1280 + c0 * 2 + part * 8;
        const bool hl = (lane & 15) < (1 << SH);
        const u32x4* wp = (const u32x4*)(p.w[br] + (c0 + 4 * part) * 4);
        u32x4 w0 = wp[0], w1 = wp[1];
        if (wave == 0 && lane < 8) cnt[lane] = 0;
        asm volatile("s_waitcnt lgkmcnt(0)" ::: "memory");
        __builtin_amdgcn_s_barrier();
        if (set == 0) __builtin_amdgcn_s_setprio(3); else __builtin_amdgcn_s_setprio(2);
        KV t0, t1;
        load_kv(t0, rk, rv, voff, (hl && rowa > 0) ? voff - 3840 : (int)0x80000000);
        __builtin_amdgcn_s_setprio(3);
        f32x2 W[2][4];
        {
            f32x2 a0 = unpack2(w0.x), a1 = unpack2(w0.y), b0 = unpack2(w0.z), b1 = unpack2(w0.w);
            W[0][0] = f32x2{a0.x, b0.x}; W[0][1] = f32x2{a0.y, b0.y}; W[0][2] = f32x2{a1.x, b1.x}; W[0][3] = f32x2{a1.y, b1.y};
            a0 = unpack2(w1.x); a1 = unpack2(w1.y); b0 = unpack2(w1.z); b1 = unpack2(w1.w);
            W[1][0] = f32x2{a0.x, b0.x}; W[1][1] = f32x2{a0.y, b0.y}; W[1][2] = f32x2{a1.x, b1.x}; W[1][3] = f32x2{a1.y, b1.y};
        }
        LAS char* xa = smem + G::LDS_X + 4 * part * G::X_CH + 2 * (L - 4 - rowa);
        convert_kv<G, (1 << SH)>(t0, W, xa);
        signal_cnt(cnt + (LO ? 0 : set), lane);
        STAMP(1);
        __builtin_amdgcn_s_setprio(1);
        load_kv(t1, rk, rv, voff + 2 * UNIT * 1280, hl ? voff + 2 * UNIT * 1280 - 3840 : (int)0x80000000);
        __builtin_amdgcn_s_setprio(3);
        convert_kv<G, (1 << SH)>(t1, W, xa - 4 * UNIT);
        signal_cnt(cnt + (LO ? 1 : 2 + set), lane);
        STAMP(2);
        __builtin_amdgcn_s_setprio(0);
        constexpr int NE = LO ? 2 : 1;
        u32x2 qv[NE][4];
#pragma unroll
        for (int i = 0; i < NE; i++)
#pragma unroll
            for (int r = 0; r < 4; r++)
                qv[i][r] = __builtin_amdgcn_raw_buffer_load_b64(rq, voff + (LO ? i : 1) * 2 * UNIT * 1280 + 1280 * r, 0, LAUX);
        const int ooff = rowa * 2560 + br * 1280 + c0 * 2 + part * 8;
#pragma unroll
        for (int i = 0; i < NE; i++) {
            const int gy = LO ? i : set;
            const int ry = LO ? rowa : rin;
            wait_cnt(cnt + 4 + gy, LO ? 16 : 8);
            STAMP(3 + 2 * i);
            LAS char* yb = smem + 4 * part * G::H_CH + 64 * (24 - 16 * gy) + ((2 * ry) ^ (part << (8 - SH)));
            u32x2 y[4];
#pragma unroll
            for (int c = 0; c < 4; c++) y[c] = *(LAS u32x2*)(yb + c * G::H_CH);
#pragma unroll
            for (int r = 0; r < 4; r++) {
                u32x2 o;
#pragma unroll
                for (int d = 0; d < 2; d++) {
                    f32x2 qf = unpack2(qv[i][r][d]);
                    f32x2 ya = unpack2(y[2 * d][r >> 1]), yc = unpack2(y[2 * d + 1][r >> 1]);
                    o[d] = pack2(qf.x * ya[r & 1], qf.y * yc[r & 1]);
                }
                __builtin_amdgcn_raw_buffer_store_b64(o, ro, ooff + (LO ? i : 1) * 2 * UNIT * 2560 + 2560 * r, 0, 0);
            }
            STAMP(4 + 2 * i);
        }
    }
    STOUT();
}

template <bool lo>
static __device__ __forceinline__ void mfma_waves(const Params& p, LAS char* smem, int br, int c0, int wave, int lane) {
    LAS u32* cnt = (LAS u32*)(smem + LDS_CNT);
    STDECL;
    STAMP(0);
    const int cw = lo ? 2 * (wave - 8) : wave - 8;
    u32x4 hv[4];
    {
        const u32x4* hp = (const u32x4*)(p.h[br] + (size_t)(c0 + cw) * S);
        const int o2 = lo ? 256 : 128;
        hv[0] = hp[lane]; hv[1] = hp[64 + lane]; hv[2] = hp[o2 + lane]; hv[3] = hp[o2 + 64 + lane];
    }
    const u32 dw = *(const __attribute__((address_space(4))) u32*)(p.dd[br] + ((c0 + cw) & ~1));
    SB();
    __builtin_amdgcn_s_barrier();
    SB();
    const int h_ch = lo ? GL::H_CH : GH::H_CH, x_ch = lo ? GL::X_CH : GH::X_CH;
    LAS char* hch = smem + cw * h_ch;
    LAS char* xch = smem + (lo ? GL::LDS_X : GH::LDS_X) + cw * x_ch;
    if (lo) {
        zero_pads<GL, 1024>(hch, xch, lane);
        zero_pads<GL, 1024>(hch + GL::H_CH, xch + GL::X_CH, lane);
        put_h<GL, 2>(hv, hch, lane);
        put_h<GL, 2>(hv + 2, hch + GL::H_CH, lane);
    } else {
        zero_pads<GH, 2048>(hch, xch, lane);
        put_h<GH, 4>(hv, hch, lane);
    }
    STAMP(1);
    const int b = lane & 15, g = lane >> 4;
    Addr A;
    {
        LAS char* hb = hch + ((b & 1) ? (h_ch >> 1) : 0);
        const int ah = b >> 1, hsw = (8 * g) ^ ((b & 1) << 4);
#pragma unroll
        for (int i = 0; i < 4; i++) { int M = ah + i; int o = 64 * M + (hsw ^ ((M & 4) << 3)); A.a[i][0] = hb + o; A.a[i][1] = hb + (o ^ 32); }
        A.x = xch + ((b & 1) ? 0 : (lo ? GL::X_BOFF : GH::X_BOFF)) + 2 * (32 - 4 * ((b + 1) >> 1) + 4 * g);
    }
    const int ysw = (cw >> 2) << (lo ? 6 : 7);
    int yo = 128 * g + 4 * b;
    f32x4 acc[4];
#pragma unroll
    for (int t = 0; t < 4; t++) acc[t] = f32x4{0.f, 0.f, 0.f, 0.f};
    bf16x8 R[8], Q[8], T0, T1, T2, X0, X1, N0, N1;
    if (!lo) {
#pragma unroll
        for (int ph = 0; ph < 8; ph++) { R[ph] = ldH(A, ph, 3); Q[ph] = ldH(A, ph, 4); }
        T0 = ldH(A, 0, 5); T1 = ldH(A, 0, 6); T2 = ldH(A, 0, 7);
        wait_cnt(cnt + 0, 4); STAMP(2);
        X0 = ldX(A, 0, 0); X1 = ldX(A, 0, 1);
#pragma unroll
        for (int ph = 0; ph < 8; ph++) {
            SB();
            acc[3] = MFMA(T2, X0, acc[3]); SB();
            T2 = ph < 7 ? ldH(A, ph + 1, 7) : ldH(A, 0, 2); SB();
            acc[2] = MFMA(T1, X0, acc[2]); SB();
            if (ph < 7) N0 = ldX(A, ph + 1, 0);
            SB();
            acc[3] = MFMA(T1, X1, acc[3]); SB();
            T1 = ph < 7 ? ldH(A, ph + 1, 6) : ldH(A, 0, 1); SB();
            acc[1] = MFMA(T0, X0, acc[1]); SB();
            if (ph < 7) N1 = ldX(A, ph + 1, 1);
            SB();
            acc[2] = MFMA(T0, X1, acc[2]); SB();
            T0 = ldH(A, (ph + 1) & 7, 5); SB();
            acc[0] = MFMA(Q[ph], X0, acc[0]);
            acc[1] = MFMA(Q[ph], X1, acc[1]);
            acc[0] = MFMA(R[ph], X1, acc[0]);
            X0 = N0; X1 = N1; PIN();
        }
        SB(); STAMP(3);
        wait_cnt(cnt + 1, 4); STAMP(4);
        X0 = ldX(A, 0, 2); X1 = ldX(A, 0, 3);
#pragma unroll
        for (int ph = 0; ph < 8; ph++) {
            SB();
            acc[3] = MFMA(T0, X0, acc[3]); SB();
            T0 = ph < 7 ? ldH(A, ph + 1, 5) : ldH(A, 0, 0); SB();
            acc[0] = MFMA(T2, X0, acc[0]); SB();
            if (ph < 7) N0 = ldX(A, ph + 1, 2);
            SB();
            acc[1] = MFMA(T2, X1, acc[1]); SB();
            T2 = ldH(A, (ph + 1) & 7, 2); SB();
            acc[2] = MFMA(Q[ph], X0, acc[2]); SB();
            if (ph < 7) N1 = ldX(A, ph + 1, 3);
            SB();
            acc[3] = MFMA(Q[ph], X1, acc[3]); SB();
            Q[ph] = T1;
            if (ph < 7) T1 = ldH(A, ph + 1, 1);
            SB();
            acc[0] = MFMA(Q[ph], X1, acc[0]);
            acc[1] = MFMA(R[ph], X0, acc[1]);
            acc[2] = MFMA(R[ph], X1, acc[2]);
            X0 = N0; X1 = N1; PIN();
        }
        SB(); STAMP(5);
    }
    LAS u32* cx = cnt + (lo ? 0 : 2);
    const u32 need = lo ? 8 : 4;
    const int nc = lo ? 2 : 1;
#pragma unroll 1
    for (int c = 0; c < nc; c++) {
        if (lo) {
#pragma unroll
            for (int ph = 0; ph < 8; ph++) { R[ph] = ldH(A, ph, 3); Q[ph] = ldH(A, ph, 1); }
            T0 = ldH(A, 0, 0); T2 = ldH(A, 0, 2);
#pragma unroll
            for (int t = 0; t < 4; t++) acc[t] = f32x4{0.f, 0.f, 0.f, 0.f};
        }
        const float dd = __builtin_bit_cast(float, ((c0 + cw + c) & 1) ? (dw & 0xffff0000u) : (dw << 16));
        LAS char* xdp = xch + (2 * (256 - 32 - 2) - yo);
        LAS char* yp0 = hch + (yo ^ ysw);
        LAS char* yp1 = hch + ((yo + 64) ^ ysw);
        u32 xd[4];
        wait_cnt(cx, need); STAMP(6);
        X0 = ldX(A, 0, 4); X1 = ldX(A, 0, 5);
#pragma unroll
        for (int ph = 0; ph < 8; ph++) {
            SB();
            acc[2] = MFMA(T2, X0, acc[2]); SB();
            if (ph < 7) N0 = ldX(A, ph + 1, 4); else load_xd<0>(xd, xdp);
            SB();
            acc[3] = MFMA(T2, X1, acc[3]); SB();
            if (ph < 7) T2 = ldH(A, ph + 1, 2);
            SB();
            acc[3] = MFMA(R[ph], X0, acc[3]); SB();
            R[ph] = T0;
            if (ph < 7) T0 = ldH(A, ph + 1, 0);
            SB();
            acc[1] = MFMA(Q[ph], X0, acc[1]); SB();
            if (ph < 7) N1 = ldX(A, ph + 1, 5);
            SB();
            acc[0] = MFMA(R[ph], X0, acc[0]);
            acc[1] = MFMA(R[ph], X1, acc[1]);
            acc[2] = MFMA(Q[ph], X1, acc[2]);
            X0 = N0; X1 = N1; PIN();
        }
        SB();
        finish_group<0>(acc, xd, yp0, yp1, dd);
        signal_cnt(cnt + 4, lane); STAMP(7);
        wait_cnt(cx + 1, need); STAMP(8);
        X0 = ldX(A, 0, 6); X1 = ldX(A, 0, 7); N0 = ldX(A, 1, 6); N1 = ldX(A, 1, 7);
#pragma unroll
        for (int ph = 0; ph < 8; ph++) {
            SB();
            acc[2] = MFMA(R[ph], X0, acc[2]); SB();
            if (ph < 6) T0 = ldX(A, ph + 2, 6);
            if (ph == 6) load_xd<1>(xd, xdp);
            SB();
            acc[3] = MFMA(Q[ph], X0, acc[3]); SB();
            if (ph < 6) T1 = ldX(A, ph + 2, 7);
            SB();
            acc[3] = MFMA(R[ph], X1, acc[3]);
            X0 = N0; X1 = N1; N0 = T0; N1 = T1; PIN();
        }
        SB();
        finish_group<1>(acc, xd, yp0, yp1, dd);
        signal_cnt(cnt + 5, lane); STAMP(9);
        hch += h_ch; xch += x_ch;
#pragma unroll
        for (int i = 0; i < 4; i++) { A.a[i][0] += h_ch; A.a[i][1] += h_ch; }
        A.x += x_ch;
    }
    STOUT();
}

#if defined(__HIP_DEVICE_COMPILE__)
#define NOPK __attribute__((target("no-packed-fp32-ops")))
#else
#define NOPK
#endif
__global__ __launch_bounds__(1024) NOPK void h3_kernel(Params p) {
    extern __shared__ __attribute__((aligned(256))) char smem_raw[];
    LAS char* smem = (LAS char*)smem_raw;
    const int tid = threadIdx.x, lane = tid & 63;
    const int wave = __builtin_amdgcn_readfirstlane(tid >> 6);
    const int bid = blockIdx.x;
    const int x = bid & 7, j = bid >> 3;
    const int br = x >> 2, cb = (x & 3) * 160;
    const bool lo = j >= 20;
    const int c0 = cb + (lo ? 16 * (j - 20) : 8 * j);
    if (wave >= 8) { if (lo) mfma_waves<true>(p, smem, br, c0, wave, lane); else mfma_waves<false>(p, smem, br, c0, wave, lane); }
    else if (lo) mem_waves<1>(p, smem, br, c0, wave, lane);
    else mem_waves<0>(p, smem, br, c0, wave, lane);
}

extern "C" void kernel_launch(void* const* d_in, const int* in_sizes, int n_in,
                              void* d_out, int out_size, void* d_ws, size_t ws_size,
                              hipStream_t stream) {
    Params p;
    for (int i = 0; i < 2; i++) {
        p.q[i] = (const u16*)d_in[6 * i + 0];
        p.k[i] = (const u16*)d_in[6 * i + 1];
        p.v[i] = (const u16*)d_in[6 * i + 2];
        p.w[i] = (const u16*)d_in[6 * i + 3];
        p.h[i] = (const u16*)d_in[6 * i + 4];
        p.dd[i] = (const u16*)d_in[6 * i + 5];
    }
    p.out = (u16*)d_out;
    (void)hipFuncSetAttribute((const void*)h3_kernel, hipFuncAttributeMaxDynamicSharedMemorySize, LDS_TOTAL);
    h3_kernel<<<240, 1024, LDS_TOTAL, stream>>>(p);
}
```

```cpp
#include <hip/hip_runtime.h>
#include <stdint.h>

typedef uint16_t u16;
typedef uint32_t u32;
typedef u32 u32x2 __attribute__((ext_vector_type(2)));
typedef u32 u32x4 __attribute__((ext_vector_type(4)));
typedef float f32x2 __attribute__((ext_vector_type(2)));
typedef float f32x4 __attribute__((ext_vector_type(4)));
typedef __bf16 bf16x2 __attribute__((ext_vector_type(2)));
typedef __bf16 bf16x8 __attribute__((ext_vector_type(8)));
#define LAS __attribute__((address_space(3)))

#define LAUX 0
#define EXACT 0

constexpr int S = 2048, D = 640;

template <int L, int C> struct Geo {
    static constexpr int H_COPY = (L / 32 + 8) * 64;
    static constexpr int H_CH = 2 * H_COPY;
    static constexpr int X_BOFF = 2 * L + 128;
    static constexpr int X_CH = X_BOFF + 2 * L + 64 + (C == 8 ? 16 : 8);
    static constexpr int LDS_X = C * H_CH;
    static constexpr int END = LDS_X + C * X_CH;
};
typedef Geo<2048, 8> GH;
typedef Geo<1024, 16> GL;
constexpr int LDS_CNT = (GH::END > GL::END ? GH::END : GL::END);
constexpr int LDS_TOTAL = LDS_CNT + 64;

struct Params {
    const u16* q[2]; const u16* k[2]; const u16* v[2]; const u16* w[2]; const u16* h[2]; const u16* dd[2];
    u16* out;
};

static __device__ __forceinline__ u32 pack2(float lo, float hi) {
    f32x2 f = {lo, hi};
    bf16x2 b = __builtin_convertvector(f, bf16x2);
    return __builtin_bit_cast(u32, b);
}
static __device__ __forceinline__ float rbf(float a) { return __builtin_bit_cast(float, pack2(0.f, a)); }
static __device__ __forceinline__ f32x2 rbf2(f32x2 a) { f32x2 r = {rbf(a.x), rbf(a.y)}; return r; }
static __device__ __forceinline__ f32x2 unpack2(u32 d) {
    f32x2 r = {__builtin_bit_cast(float, d << 16), __builtin_bit_cast(float, d & 0xffff0000u)};
    return r;
}
static __device__ __forceinline__ void wait_cnt(LAS u32* c, u32 n) {
    u32 tv, ts, tc;
    asm volatile(
        "s_mov_b32 %2, 0x400000\n"
        "W%=:\n\t"
        "ds_read_b32 %0, %3\n\t"
        "s_waitcnt lgkmcnt(0)\n\t"
        "v_readfirstlane_b32 %1, %0\n\t"
        "s_cmp_ge_u32 %1, %4\n\t"
        "s_cbranch_scc1 E%=\n\t"
        "s_sleep 1\n\t"
        "s_sub_u32 %2, %2, 1\n\t"
        "s_cbranch_scc0 W%=\n"
        "E%=:"
        : "=&v"(tv), "=&s"(ts), "=&s"(tc) : "v"(c), "s"(n) : "memory", "scc");
}
static __device__ __forceinline__ void signal_cnt(LAS u32* c, int lane) {
    u32 one = 1;
    unsigned long long sv;
    asm volatile("s_mov_b64 %0, exec\n\ts_mov_b64 exec, 1\n\tds_add_u32 %1, %2\n\ts_mov_b64 exec, %0"
                 : "=&s"(sv) : "v"(c), "v"(one) : "memory");
}
#define STAMP(i)
#define STDECL
#define STOUT()

typedef volatile LAS u32x2* vl64;
static __device__ __forceinline__ bf16x8 ldfrag(LAS char* a0, LAS char* a1, int off) {
    u32x2 lo = *(vl64)(a0 + off);
    u32x2 hi = *(vl64)(a1 + off);
    u32x4 r = {lo.x, lo.y, hi.x, hi.y};
    return __builtin_bit_cast(bf16x8, r);
}

struct KV { u32x2 k[4], v[4], halo[3]; };

template <typename RS>
static __device__ __forceinline__ void load_kv(KV& t, RS rk, RS rv, int voff, int hoff) {
#pragma unroll
    for (int r = 0; r < 4; r++) t.k[r] = __builtin_amdgcn_raw_buffer_load_b64(rk, voff + 1280 * r, 0, LAUX);
#pragma unroll
    for (int r = 0; r < 3; r++) t.halo[r] = __builtin_amdgcn_raw_buffer_load_b64(rk, hoff + 1280 * r, 0, LAUX);
#pragma unroll
    for (int r = 0; r < 4; r++) t.v[r] = __builtin_amdgcn_raw_buffer_load_b64(rv, voff + 1280 * r, 0, LAUX);
}

template <typename G, int SHR>
static __device__ __forceinline__ void convert_kv(const KV& t, const f32x2 (&W)[2][4], LAS char* xa) {
#pragma unroll
    for (int d = 0; d < 2; d++) {
        f32x2 kk[7];
#pragma unroll
        for (int r = 0; r < 3; r++)
            kk[r] = unpack2(__builtin_amdgcn_update_dpp(t.halo[r][d], t.k[r + 1][d], 0x110 + SHR, 0xf, 0xf, false));
#pragma unroll
        for (int r = 0; r < 4; r++) kk[3 + r] = unpack2(t.k[r][d]);
        f32x2 x[4];
#pragma unroll
        for (int r = 0; r < 4; r++) {
            f32x2 a = kk[r] * W[d][3];
            a = __builtin_elementwise_fma(kk[r + 1], W[d][2], a);
            a = __builtin_elementwise_fma(kk[r + 2], W[d][1], a);
            a = __builtin_elementwise_fma(kk[r + 3], W[d][0], a);
            x[r] = a * unpack2(t.v[r][d]);
        }
#pragma unroll
        for (int c = 0; c < 2; c++) {
            u32 d0 = pack2(x[3][c], x[2][c]), d1 = pack2(x[1][c], x[0][c]);
            LAS char* a = xa + (2 * d + c) * G::X_CH;
            u32x2 dd = {d0, d1};
            *(LAS u32x2*)a = dd;
            *(LAS u32*)(a + G::X_BOFF + 4) = d0;
            *(LAS u32*)(a + G::X_BOFF + 8) = d1;
        }
    }
}

#define MFMA(a, b, c) __builtin_amdgcn_mfma_f32_16x16x32_bf16(a, b, c, 0, 0, 0)
#define SB() __builtin_amdgcn_sched_barrier(0)
#define PIN() asm volatile("" : "+v"(acc[0]), "+v"(acc[1]), "+v"(acc[2]), "+v"(acc[3]))
struct Addr { LAS char* a[4][2]; LAS char* x; };
static __device__ __forceinline__ bf16x8 ldH(const Addr& A, int ph, int mu) {
    return (ph & 4) ? ldfrag(A.a[ph & 3][1], A.a[ph & 3][0], 256 + 512 * mu) : ldfrag(A.a[ph & 3][0], A.a[ph & 3][1], 512 * mu);
}
static __device__ __forceinline__ bf16x8 ldX(const Addr& A, int ph, int p) { return ldfrag(A.x, A.x + 32, 64 * ph + 512 * (7 - p)); }

template <int GI>
static __device__ __forceinline__ void load_xd(u32 (&xd)[4], LAS char* xdp) {
#pragma unroll
    for (int i = 0; i < 2; i++)
#pragma unroll
        for (int rp = 0; rp < 2; rp++) xd[2 * i + rp] = *(volatile LAS u32*)(xdp + 2 * (768 + 32 - 512 * GI - 256 * i - 32 * rp));
}
template <int GI>
static __device__ __forceinline__ void finish_group(const f32x4 (&acc)[4], const u32 (&xd)[4], LAS char* yp0, LAS char* yp1, float dd) {
#pragma unroll
    for (int i = 0; i < 2; i++) {
#pragma unroll
        for (int rp = 0; rp < 2; rp++) {
            f32x2 xf = unpack2(xd[2 * i + rp]);
            const f32x4& a = acc[2 * GI + i];
            float y0, y1;
            y0 = __builtin_fmaf(xf.y, dd, a[2 * rp]);
            y1 = __builtin_fmaf(xf.x, dd, a[2 * rp + 1]);
            *(LAS u32*)((rp ? yp1 : yp0) + 64 * (24 - 16 * GI) + 512 * i) = pack2(y0, y1);
        }
    }
}

template <typename G, int NR>
static __device__ __forceinline__ void put_h(const u32x4* hv, LAS char* hch, int lane) {
    u32 prev = 0;
#pragma unroll
    for (int r = 0; r < NR; r++) {
        u32 pl = __builtin_amdgcn_update_dpp(prev, hv[r].w, 0x138, 0xf, 0xf, false);
        prev = __builtin_amdgcn_readlane(hv[r].w, 63);
        u32x4 s;
        s.x = __builtin_amdgcn_alignbit(hv[r].x, pl, 16);
        s.y = __builtin_amdgcn_alignbit(hv[r].y, hv[r].x, 16);
        s.z = __builtin_amdgcn_alignbit(hv[r].z, hv[r].y, 16);
        s.w = __builtin_amdgcn_alignbit(hv[r].w, hv[r].z, 16);
        int u = 28 + 64 * r + lane;
        int o = 16 * (u ^ ((u >> 3) & 2));
        *(LAS u32x4*)(hch + o) = s;
        *(LAS u32x4*)(hch + G::H_COPY + (o ^ 16)) = hv[r];
    }
}
template <typename G, int L>
static __device__ __forceinline__ void zero_pads(LAS char* hch, LAS char* xch, int lane) {
    u32x4 z = {0, 0, 0, 0};
    if (lane < 56) *(LAS u32x4*)(hch + (lane >= 28 ? G::H_COPY - 448 : 0) + 16 * lane) = z;
    if (lane < 32) *(LAS u32*)(xch + 2 * L + (lane >= 16 ? G::X_BOFF - 60 : 0) + 4 * lane) = 0;
}

template <int LO>
static __device__ __forceinline__ void mem_waves(const Params& p, LAS char* smem, int br, int c0, int wave, int lane) {
    typedef Geo<LO ? 1024 : 2048, LO ? 16 : 8> G;
    constexpr int L = LO ? 1024 : 2048;
    LAS u32* cnt = (LAS u32*)(smem + LDS_CNT);
    STDECL;
    STAMP(0);
    {
        constexpr int SH = LO ? 2 : 1;
        constexpr int UNIT = L / 4;
        const int set = wave >> 2, wq = wave & 3;
        const int quad = lane >> SH, part = lane & ((1 << SH) - 1);
        auto rk = __builtin_amdgcn_make_buffer_rsrc((void*)p.k[br], 0, S * D * 2, 0x00020000);
        auto rv = __builtin_amdgcn_make_buffer_rsrc((void*)p.v[br], 0, S * D * 2, 0x00020000);
        auto rq = __builtin_amdgcn_make_buffer_rsrc((void*)p.q[br], 0, S * D * 2, 0x00020000);
        auto ro = __builtin_amdgcn_make_buffer_rsrc((void*)p.out, 0, S * D * 4, 0x00020000);
        const int rin = (UNIT / 4) * wq + 4 * quad;
        const int rowa = UNIT * set + rin;
        const int voff = rowa * 1280 + c0 * 2 + part * 8;
        const bool hl = (lane & 15) < (1 << SH);
        const u32x4* wp = (const u32x4*)(p.w[br] + (c0 + 4 * part) * 4);
        u32x4 w0 = wp[0], w1 = wp[1];
        if (wave == 0 && lane < 8) cnt[lane] = 0;
        asm volatile("s_waitcnt lgkmcnt(0)" ::: "memory");
        __builtin_amdgcn_s_barrier();
        if (set == 0) __builtin_amdgcn_s_setprio(3); else __builtin_amdgcn_s_setprio(2);
        KV t0, t1;
        load_kv(t0, rk, rv, voff, (hl && rowa > 0) ? voff - 3840 : (int)0x80000000);
        __builtin_amdgcn_s_setprio(3);
        f32x2 W[2][4];
        {
            f32x2 a0 = unpack2(w0.x), a1 = unpack2(w0.y), b0 = unpack2(w0.z), b1 = unpack2(w0.w);
            W[0][0] = f32x2{a0.x, b0.x}; W[0][1] = f32x2{a0.y, b0.y}; W[0][2] = f32x2{a1.x, b1.x}; W[0][3] = f32x2{a1.y, b1.y};
            a0 = unpack2(w1.x); a1 = unpack2(w1.y); b0 = unpack2(w1.z); b1 = unpack2(w1.w);
            W[1][0] = f32x2{a0.x, b0.x}; W[1][1] = f32x2{a0.y, b0.y}; W[1][2] = f32x2{a1.x, b1.x}; W[1][3] = f32x2{a1.y, b1.y};
        }
        LAS char* xa = smem + G::LDS_X + 4 * part * G::X_CH + 2 * (L - 4 - rowa);
        convert_kv<G, (1 << SH)>(t0, W, xa);
        signal_cnt(cnt + (LO ? 0 : set), lane);
        STAMP(1);
        __builtin_amdgcn_s_setprio(1);
        load_kv(t1, rk, rv, voff + 2 * UNIT * 1280, hl ? voff + 2 * UNIT * 1280 - 3840 : (int)0x80000000);
        __builtin_amdgcn_s_setprio(3);
        convert_kv<G, (1 << SH)>(t1, W, xa - 4 * UNIT);
        signal_cnt(cnt + (LO ? 1 : 2 + set), lane);
        STAMP(2);
        __builtin_amdgcn_s_setprio(0);
        constexpr int NE = LO ? 2 : 1;
        u32x2 qv[NE][4];
#pragma unroll
        for (int i = 0; i < NE; i++)
#pragma unroll
            for (int r = 0; r < 4; r++)
                qv[i][r] = __builtin_amdgcn_raw_buffer_load_b64(rq, voff + (LO ? i : 1) * 2 * UNIT * 1280 + 1280 * r, 0, LAUX);
        const int ooff = rowa * 2560 + br * 1280 + c0 * 2 + part * 8;
#pragma unroll
        for (int i = 0; i < NE; i++) {
            const int gy = LO ? i : set;
            const int ry = LO ? rowa : rin;
            wait_cnt(cnt + 4 + gy, LO ? 16 : 8);
            STAMP(3 + 2 * i);
            LAS char* yb = smem + 4 * part * G::H_CH + 64 * (24 - 16 * gy) + ((2 * ry) ^ (part << (8 - SH)));
            u32x2 y[4];
#pragma unroll
            for (int c = 0; c < 4; c++) y[c] = *(LAS u32x2*)(yb + c * G::H_CH);
#pragma unroll
            for (int r = 0; r < 4; r++) {
                u32x2 o;
#pragma unroll
                for (int d = 0; d < 2; d++) {
                    f32x2 qf = unpack2(qv[i][r][d]);
                    f32x2 ya = unpack2(y[2 * d][r >> 1]), yc = unpack2(y[2 * d + 1][r >> 1]);
                    o[d] = pack2(qf.x * ya[r & 1], qf.y * yc[r & 1]);
                }
                __builtin_amdgcn_raw_buffer_store_b64(o, ro, ooff + (LO ? i : 1) * 2 * UNIT * 2560 + 2560 * r, 0, 0);
            }
            STAMP(4 + 2 * i);
        }
    }
    STOUT();
}

template <bool lo>
static __device__ __forceinline__ void mfma_waves(const Params& p, LAS char* smem, int br, int c0, int wave, int lane) {
    LAS u32* cnt = (LAS u32*)(smem + LDS_CNT);
    STDECL;
    STAMP(0);
    const int cw = lo ? 2 * (wave - 8) : wave - 8;
    u32x4 hv[4];
    {
        const u32x4* hp = (const u32x4*)(p.h[br] + (size_t)(c0 + cw) * S);
        const int o2 = lo ? 256 : 128;
        hv[0] = hp[lane]; hv[1] = hp[64 + lane]; hv[2] = hp[o2 + lane]; hv[3] = hp[o2 + 64 + lane];
    }
    const u32 dw = *(const __attribute__((address_space(4))) u32*)(p.dd[br] + ((c0 + cw) & ~1));
    SB();
    __builtin_amdgcn_s_barrier();
    SB();
    const int h_ch = lo ? GL::H_CH : GH::H_CH, x_ch = lo ? GL::X_CH : GH::X_CH;
    LAS char* hch = smem + cw * h_ch;
    LAS char* xch = smem + (lo ? GL::LDS_X : GH::LDS_X) + cw * x_ch;
    if (lo) {
        zero_pads<GL, 1024>(hch, xch, lane);
        zero_pads<GL, 1024>(hch + GL::H_CH, xch + GL::X_CH, lane);
        put_h<GL, 2>(hv, hch, lane);
        put_h<GL, 2>(hv + 2, hch + GL::H_CH, lane);
    } else {
        zero_pads<GH, 2048>(hch, xch, lane);
        put_h<GH, 4>(hv, hch, lane);
    }
    STAMP(1);
    const int b = lane & 15, g = lane >> 4;
    Addr A;
    {
        LAS char* hb = hch + ((b & 1) ? (h_ch >> 1) : 0);
        const int ah = b >> 1, hsw = (8 * g) ^ ((b & 1) << 4);
#pragma unroll
        for (int i = 0; i < 4; i++) { int M = ah + i; int o = 64 * M + (hsw ^ ((M & 4) << 3)); A.a[i][0] = hb + o; A.a[i][1] = hb + (o ^ 32); }
        A.x = xch + ((b & 1) ? 0 : (lo ? GL::X_BOFF : GH::X_BOFF)) + 2 * (32 - 4 * ((b + 1) >> 1) + 4 * g);
    }
    const int ysw = (cw >> 2) << (lo ? 6 : 7);
    int yo = 128 * g + 4 * b;
    f32x4 acc[4];
#pragma unroll
    for (int t = 0; t < 4; t++) acc[t] = f32x4{0.f, 0.f, 0.f, 0.f};
    bf16x8 R[8], Q[8], T0, T1, T2, X0, X1, N0, N1;
    if (!lo) {
#pragma unroll
        for (int ph = 0; ph < 8; ph++) { R[ph] = ldH(A, ph, 3); Q[ph] = ldH(A, ph, 4); }
        T0 = ldH(A, 0, 5); T1 = ldH(A, 0, 6); T2 = ldH(A, 0, 7);
        wait_cnt(cnt + 0, 4); STAMP(2);
        X0 = ldX(A, 0, 0); X1 = ldX(A, 0, 1);
#pragma unroll
        for (int ph = 0; ph < 8; ph++) {
            SB();
            acc[3] = MFMA(T2, X0, acc[3]); SB();
            T2 = ph < 7 ? ldH(A, ph + 1, 7) : ldH(A, 0, 2); SB();
            acc[2] = MFMA(T1, X0, acc[2]); SB();
            if (ph < 7) N0 = ldX(A, ph + 1, 0);
            SB();
            acc[3] = MFMA(T1, X1, acc[3]); SB();
            T1 = ph < 7 ? ldH(A, ph + 1, 6) : ldH(A, 0, 1); SB();
            acc[1] = MFMA(T0, X0, acc[1]); SB();
            if (ph < 7) N1 = ldX(A, ph + 1, 1);
            SB();
            acc[2] = MFMA(T0, X1, acc[2]); SB();
            T0 = ldH(A, (ph + 1) & 7, 5); SB();
            acc[0] = MFMA(Q[ph], X0, acc[0]);
            acc[1] = MFMA(Q[ph], X1, acc[1]);
            acc[0] = MFMA(R[ph], X1, acc[0]);
            X0 = N0; X1 = N1; PIN();
        }
        SB(); STAMP(3);
        wait_cnt(cnt + 1, 4); STAMP(4);
        X0 = ldX(A, 0, 2); X1 = ldX(A, 0, 3);
#pragma unroll
        for (int ph = 0; ph < 8; ph++) {
            SB();
            acc[3] = MFMA(T0, X0, acc[3]); SB();
            T0 = ph < 7 ? ldH(A, ph + 1, 5) : ldH(A, 0, 0); SB();
            acc[0] = MFMA(T2, X0, acc[0]); SB();
            if (ph < 7) N0 = ldX(A, ph + 1, 2);
            SB();
            acc[1] = MFMA(T2, X1, acc[1]); SB();
            T2 = ldH(A, (ph + 1) & 7, 2); SB();
            acc[2] = MFMA(Q[ph], X0, acc[2]); SB();
            if (ph < 7) N1 = ldX(A, ph + 1, 3);
            SB();
            acc[3] = MFMA(Q[ph], X1, acc[3]); SB();
            Q[ph] = T1;
            if (ph < 7) T1 = ldH(A, ph + 1, 1);
            SB();
            acc[0] = MFMA(Q[ph], X1, acc[0]);
            acc[1] = MFMA(R[ph], X0, acc[1]);
            acc[2] = MFMA(R[ph], X1, acc[2]);
            X0 = N0; X1 = N1; PIN();
        }
        SB(); STAMP(5);
    }
    LAS u32* cx = cnt + (lo ? 0 : 2);
    const u32 need = lo ? 8 : 4;
    const int nc = lo ? 2 : 1;
#pragma unroll 1
    for (int c = 0; c < nc; c++) {
        if (lo) {
#pragma unroll
            for (int ph = 0; ph < 8; ph++) { R[ph] = ldH(A, ph, 3); Q[ph] = ldH(A, ph, 1); }
            T0 = ldH(A, 0, 0); T2 = ldH(A, 0, 2);
#pragma unroll
            for (int t = 0; t < 4; t++) acc[t] = f32x4{0.f, 0.f, 0.f, 0.f};
        }
        const float dd = __builtin_bit_cast(float, ((c0 + cw + c) & 1) ? (dw & 0xffff0000u) : (dw << 16));
        LAS char* xdp = xch + (2 * (256 - 32 - 2) - yo);
        LAS char* yp0 = hch + (yo ^ ysw);
        LAS char* yp1 = hch + ((yo + 64) ^ ysw);
        u32 xd[4];
        wait_cnt(cx, need); STAMP(6);
        X0 = ldX(A, 0, 4); X1 = ldX(A, 0, 5);
#pragma unroll
        for (int ph = 0; ph < 8; ph++) {
            SB();
            acc[2] = MFMA(T2, X0, acc[2]); SB();
            if (ph < 7) N0 = ldX(A, ph + 1, 4); else load_xd<0>(xd, xdp);
            SB();
            acc[3] = MFMA(T2, X1, acc[3]); SB();
            if (ph < 7) T2 = ldH(A, ph + 1, 2);
            SB();
            acc[3] = MFMA(R[ph], X0, acc[3]); SB();
            R[ph] = T0;
            if (ph < 7) T0 = ldH(A, ph + 1, 0);
            SB();
            acc[1] = MFMA(Q[ph], X0, acc[1]); SB();
            if (ph < 7) N1 = ldX(A, ph + 1, 5);
            SB();
            acc[0] = MFMA(R[ph], X0, acc[0]);
            acc[1] = MFMA(R[ph], X1, acc[1]);
            acc[2] = MFMA(Q[ph], X1, acc[2]);
            X0 = N0; X1 = N1; PIN();
        }
        SB();
        finish_group<0>(acc, xd, yp0, yp1, dd);
        signal_cnt(cnt + 4, lane); STAMP(7);
        wait_cnt(cx + 1, need); STAMP(8);
        X0 = ldX(A, 0, 6); X1 = ldX(A, 0, 7); N0 = ldX(A, 1, 6); N1 = ldX(A, 1, 7);
#pragma unroll
        for (int ph = 0; ph < 8; ph++) {
            SB();
            acc[2] = MFMA(R[ph], X0, acc[2]); SB();
            if (ph < 6) T0 = ldX(A, ph + 2, 6);
            if (ph == 6) load_xd<1>(xd, xdp);
            SB();
            acc[3] = MFMA(Q[ph], X0, acc[3]); SB();
            if (ph < 6) T1 = ldX(A, ph + 2, 7);
            SB();
            acc[3] = MFMA(R[ph], X1, acc[3]);
            X0 = N0; X1 = N1; N0 = T0; N1 = T1; PIN();
        }
        SB();
        finish_group<1>(acc, xd, yp0, yp1, dd);
        signal_cnt(cnt + 5, lane); STAMP(9);
        hch += h_ch; xch += x_ch;
#pragma unroll
        for (int i = 0; i < 4; i++) { A.a[i][0] += h_ch; A.a[i][1] += h_ch; }
        A.x += x_ch;
    }
    STOUT();
}

#define PROBE 1
static __device__ __forceinline__ u32 probe(const Params& p, int bid, int tid) {
    u32 acc = 0;
    const int x = bid & 7, j = bid >> 3, br = x >> 2, xoff = (x & 3) * 320;
    auto rk = __builtin_amdgcn_make_buffer_rsrc((void*)p.k[br], 0, S * D * 2, 0x00020000);
    auto rv = __builtin_amdgcn_make_buffer_rsrc((void*)p.v[br], 0, S * D * 2, 0x00020000);
    auto rq = __builtin_amdgcn_make_buffer_rsrc((void*)p.q[br], 0, S * D * 2, 0x00020000);
    auto rh = __builtin_amdgcn_make_buffer_rsrc((void*)p.h[br], 0, S * D * 2, 0x00020000);
    if (PROBE == 2) { acc = __builtin_amdgcn_raw_buffer_load_b32(rk, ((x & 3) * 30 + j) * 21760 + (tid & 63) * 4, 0, 0); }
    if (PROBE == 3 || PROBE == 4) {
        constexpr int AUX = PROBE == 4 ? 2 : 0;
        int base = ((x & 3) * 30 + j) * 1366;
        u32x4 a[8];
#pragma unroll
        for (int r = 0; r < 2; r++) {
            int i = r * 1024 + tid; int o = i < 1366 ? (base + i) * 16 : 0x80000000;
            a[r * 4 + 0] = __builtin_amdgcn_raw_buffer_load_b128(rk, o, 0, AUX);
            a[r * 4 + 1] = __builtin_amdgcn_raw_buffer_load_b128(rv, o, 0, AUX);
            a[r * 4 + 2] = __builtin_amdgcn_raw_buffer_load_b128(rq, o, 0, AUX);
            a[r * 4 + 3] = __builtin_amdgcn_raw_buffer_load_b128(rh, o, 0, AUX);
        }
#pragma unroll
        for (int r = 0; r < 8; r++) acc ^= a[r].x ^ a[r].w;
    }
    if (PROBE == 6 || PROBE == 7) {
        constexpr int AUX = PROBE == 6 ? 2 : 0;
        int rr = tid / 3, c = tid % 3; int row = j + 30 * rr;
        int o = row < 2048 ? row * 1280 + xoff + (c == 0 ? 0 : c == 1 ? 128 : 316) : 0x80000000;
        acc ^= __builtin_amdgcn_raw_buffer_load_b32(rk, o, 0, AUX);
        acc ^= __builtin_amdgcn_raw_buffer_load_b32(rv, o, 0, AUX);
        asm volatile("" :: "v"(acc));
        __syncthreads();
    }
    if (PROBE == 5 || PROBE == 6 || PROBE == 7) {
        if (j < 20) {
            u32x4 a[2], c[2];
#pragma unroll
            for (int r = 0; r < 2; r++) {
                int row = r * 1024 + tid;
                a[r] = __builtin_amdgcn_raw_buffer_load_b128(rk, row * 1280 + xoff + j * 16, 0, 0);
                c[r] = __builtin_amdgcn_raw_buffer_load_b128(rv, row * 1280 + xoff + j * 16, 0, 0);
            }
            acc ^= a[0].x ^ a[1].y ^ c[0].z ^ c[1].w;
        }
    }
    asm volatile("" :: "v"(acc));
    __syncthreads();
    return acc;
}
#if defined(__HIP_DEVICE_COMPILE__)
#define NOPK __attribute__((target("no-packed-fp32-ops")))
#else
#define NOPK
#endif
__global__ __launch_bounds__(1024) NOPK void h3_kernel(Params p) {
    extern __shared__ __attribute__((aligned(256))) char smem_raw[];
    LAS char* smem = (LAS char*)smem_raw;
    const int tid = threadIdx.x, lane = tid & 63;
    const int wave = __builtin_amdgcn_readfirstlane(tid >> 6);
    const int bid = blockIdx.x;
    unsigned long long pt0 = __builtin_amdgcn_s_memrealtime();
    probe(p, bid, tid);
    unsigned long long pt1 = __builtin_amdgcn_s_memrealtime();
    const int x = bid & 7, j = bid >> 3;
    const int br = x >> 2, cb = (x & 3) * 160;
    const bool lo = j >= 20;
    const int c0 = cb + (lo ? 16 * (j - 20) : 8 * j);
    if (wave >= 8) { if (lo) mfma_waves<true>(p, smem, br, c0, wave, lane); else mfma_waves<false>(p, smem, br, c0, wave, lane); }
    else if (lo) mem_waves<1>(p, smem, br, c0, wave, lane);
    else mem_waves<0>(p, smem, br, c0, wave, lane);
    if (wave == 0) { while (__builtin_amdgcn_s_memrealtime() - pt1 < 900) __builtin_amdgcn_s_sleep(1); }
}

extern "C" void kernel_launch(void* const* d_in, const int* in_sizes, int n_in,
                              void* d_out, int out_size, void* d_ws, size_t ws_size,
                              hipStream_t stream) {
    Params p;
    for (int i = 0; i < 2; i++) {
        p.q[i] = (const u16*)d_in[6 * i + 0];
        p.k[i] = (const u16*)d_in[6 * i + 1];
        p.v[i] = (const u16*)d_in[6 * i + 2];
        p.w[i] = (const u16*)d_in[6 * i + 3];
        p.h[i] = (const u16*)d_in[6 * i + 4];
        p.dd[i] = (const u16*)d_in[6 * i + 5];
    }
    p.out = (u16*)d_out;
    (void)hipFuncSetAttribute((const void*)h3_kernel, hipFuncAttributeMaxDynamicSharedMemorySize, LDS_TOTAL);
    h3_kernel<<<240, 1024, LDS_TOTAL, stream>>>(p);
}
```

```cpp
#include <hip/hip_runtime.h>
#include <stdint.h>

typedef uint16_t u16;
typedef uint32_t u32;
typedef u32 u32x2 __attribute__((ext_vector_type(2)));
typedef u32 u32x4 __attribute__((ext_vector_type(4)));
typedef float f32x2 __attribute__((ext_vector_type(2)));
typedef float f32x4 __attribute__((ext_vector_type(4)));
typedef __bf16 bf16x2 __attribute__((ext_vector_type(2)));
typedef __bf16 bf16x8 __attribute__((ext_vector_type(8)));
#define LAS __attribute__((address_space(3)))
#define CAS __attribute__((address_space(4)))
#define INL __attribute__((always_inline))
#define SB() __builtin_amdgcn_sched_barrier(0)

constexpr int S = 2048, D = 640;
constexpr int OOB = (int)0x80000000;

constexpr int TR = 64, TM = 192, NSP = 3;

constexpr int H_COPY = 2112, H_W = 4 * H_COPY, H_HEAD = 48, LDS_X = 8 * H_W;
template <int LO> struct Geo {
    static constexpr int L = LO ? 1024 : 2048, C = LO ? 16 : 8, NB = L / 256;
    static constexpr int X_BOFF = 2 * L + 464;
    static constexpr int X_CH = 2 * X_BOFF + (LO ? 16 : 48);
    static constexpr int END = LDS_X + C * X_CH;
};
constexpr int LDS_CNT = Geo<1>::END;
constexpr int LDS_JUNK = LDS_CNT + 64;
constexpr int LDS_TOTAL = LDS_JUNK + 256;
static_assert(Geo<0>::END <= LDS_CNT && LDS_TOTAL <= 163840, "LDS");

struct Ptrs { const u16 *q, *k, *v, *w, *h, *dd; u16* out; u16* out2; };
struct Params { Ptrs b[2]; };

static __device__ __forceinline__ u32 pack2(float lo, float hi) {
    f32x2 f = {lo, hi};
    bf16x2 b = __builtin_convertvector(f, bf16x2);
    return __builtin_bit_cast(u32, b);
}
static __device__ __forceinline__ f32x2 unpack2(u32 d) {
    f32x2 r = {__builtin_bit_cast(float, d << 16), __builtin_bit_cast(float, d & 0xffff0000u)};
    return r;
}
static __device__ __forceinline__ int fresh_lane() {
    int l;
    asm volatile("v_mbcnt_lo_u32_b32 %0, -1, 0\n\tv_mbcnt_hi_u32_b32 %0, -1, %0" : "=v"(l));
    return l;
}

template <int SL>
static __device__ __forceinline__ void wait_from(LAS u32* c, u32 n, u32 v) {
    u32 ts, tc;
    asm volatile(
        "s_mov_b32 %2, 0x400000\n"
        "W%=:\n\t"
        "v_readfirstlane_b32 %1, %0\n\t"
        "s_cmp_ge_u32 %1, %4\n\t"
        "s_cbranch_scc1 E%=\n\t"
        "s_sleep %5\n\t"
        "ds_read_b32 %0, %3\n\t"
        "s_sub_u32 %2, %2, 1\n\t"
        "s_waitcnt lgkmcnt(0)\n\t"
        "s_cbranch_scc0 W%=\n"
        "E%=:"
        : "+v"(v), "=&s"(ts), "=&s"(tc) : "v"(c), "s"(n), "i"(SL) : "memory", "scc");
}
static __device__ __forceinline__ u32 peek(LAS u32* c) { return *(volatile LAS u32*)c; }
template <int SL>
static __device__ __forceinline__ void wait_cnt(LAS u32* c, u32 n) { wait_from<SL>(c, n, peek(c)); }
static __device__ __forceinline__ void signal_cnt(LAS u32* c) {
    u32 one;
    asm volatile("s_mov_b64 exec, 1\n\tv_mov_b32 %0, 1\n\tds_add_u32 %1, %0\n\ts_mov_b64 exec, -1"
                 : "=&v"(one) : "v"(c) : "memory");
}

template <int ND> struct Row { u32 d[ND]; };
template <int ND, typename RS>
static __device__ __forceinline__ Row<ND> ldrow(RS r, int off, int so) {
    Row<ND> o;
    if constexpr (ND == 1) o.d[0] = __builtin_amdgcn_raw_buffer_load_b32(r, off, so, 0);
    else { u32x2 t = __builtin_amdgcn_raw_buffer_load_b64(r, off, so, 0); o.d[0] = t.x; o.d[1] = t.y; }
    return o;
}
template <int ND> struct KV { Row<ND> k[4], v[4], halo[3]; };
template <int ND, typename RS>
static __device__ __forceinline__ void load_kv(KV<ND>& t, RS rk, RS rv, int voff, int so, int ho, int hso) {
#pragma unroll
    for (int r = 0; r < 4; r++) t.k[r] = ldrow<ND>(rk, voff + 1280 * r, so);
#pragma unroll
    for (int r = 0; r < 3; r++) t.halo[r] = ldrow<ND>(rk, ho + 1280 * r, hso);
#pragma unroll
    for (int r = 0; r < 4; r++) t.v[r] = ldrow<ND>(rv, voff + 1280 * r, so);
}
template <int ND, typename G>
static __device__ __forceinline__ void convert_kv(const KV<ND>& t, const f32x2 (&W)[ND][4], LAS char* xa, int lane) {
    u32 pv[ND][3];
#pragma unroll
    for (int d = 0; d < ND; d++)
#pragma unroll
        for (int r = 0; r < 3; r++) {
            const u32 n = __builtin_amdgcn_ds_bpermute(4 * lane - 16, t.k[1 + r].d[d]);
            pv[d][r] = lane < 4 ? t.halo[r].d[d] : n;
        }
#pragma unroll
    for (int d = 0; d < ND; d++) {
        f32x2 kk[7];
#pragma unroll
        for (int r = 0; r < 4; r++) kk[3 + r] = unpack2(t.k[r].d[d]);
#pragma unroll
        for (int r = 0; r < 3; r++) kk[r] = unpack2(pv[d][r]);
        f32x2 x[4];
#pragma unroll
        for (int r = 3; r >= 0; r--) {
            f32x2 a = kk[r + 3] * W[d][0];
            a = __builtin_elementwise_fma(kk[r + 2], W[d][1], a);
            a = __builtin_elementwise_fma(kk[r + 1], W[d][2], a);
            a = __builtin_elementwise_fma(kk[r], W[d][3], a);
            x[r] = a * unpack2(t.v[r].d[d]);
        }
#pragma unroll
        for (int c = 0; c < 2; c++) {
            const u32x2 dd = {pack2(x[3][c], x[2][c]), pack2(x[1][c], x[0][c])};
            LAS char* a = xa + (2 * d + c) * G::X_CH;
            *(volatile LAS u32x2*)a = dd;
            *(volatile LAS u32x2*)(a + G::X_BOFF + 8) = dd;
        }
    }
}

template <int LO>
static __device__ __forceinline__ void mem_waves(const Ptrs& p, LAS char* smem, int br, int c0, int wave, int j, int cb) {
    const int lane = fresh_lane();
    typedef Geo<LO> G;
    constexpr int ND = LO ? 2 : 1, NB = G::NB, QE = 4;
    LAS u32* cnt = (LAS u32*)(smem + LDS_CNT);
    const int quad = lane >> 2, part = lane & 3;
    auto rk = __builtin_amdgcn_make_buffer_rsrc((void*)p.k, 0, S * D * 2, 0x00020000);
    auto rv = __builtin_amdgcn_make_buffer_rsrc((void*)p.v, 0, S * D * 2, 0x00020000);
    const int rin = 64 * wave + 4 * quad;
    const int voff = rin * 1280 + c0 * 2 + part * 4 * ND;
    const bool hl = lane < 4;
    u32x4 wv[ND];
#pragma unroll
    for (int d = 0; d < ND; d++) wv[d] = *(const u32x4*)(p.w + (c0 + 2 * ND * part + 2 * d) * 4);
    const int hoff = hl ? voff : OOB;
    KV<ND> t[NB];
    if (!LO) { __builtin_amdgcn_s_setprio(2); __builtin_amdgcn_s_sleep(8); }
    load_kv<ND>(t[0], rk, rv, voff, 0, (hl && rin > 0) ? voff - 3840 : OOB, 0);
    f32x2 W[ND][4];
#pragma unroll
    for (int d = 0; d < ND; d++) {
        f32x2 a0 = unpack2(wv[d].x), a1 = unpack2(wv[d].y), b0 = unpack2(wv[d].z), b1 = unpack2(wv[d].w);
        W[d][0] = f32x2{a0.x, b0.x}; W[d][1] = f32x2{a0.y, b0.y}; W[d][2] = f32x2{a1.x, b1.x}; W[d][3] = f32x2{a1.y, b1.y};
    }
    LAS char* xa = smem + LDS_X + 2 * ND * part * G::X_CH + 2 * (G::L - 4 - rin);
    auto rq = __builtin_amdgcn_make_buffer_rsrc((void*)p.q, 0, S * D * 2, 0x00020000);
    constexpr int R0 = LO ? 0 : 1024;
    Row<ND> qv[4][4];
    auto load_q1 = [&](int i) INL { qv[i >> 2][i & 3] = ldrow<ND>(rq, voff + 1280 * (i & 3), (R0 + 256 * (i >> 2)) * 1280); };
#pragma unroll
    for (int u = 0; u < NB; u++) {
        SB();
        if (!LO && u == 1) {
            const int r = ((lane & 31) * 11) >> 5, c = (lane & 31) - 3 * r;
            const int rr = j + 20 * r;
            const int o = (rr < TM && r < 10) ? ((8 * TR + TM * wave + rr) * D + cb + c * 64 + (c >> 1) * 30) * 2 : OOB;
            asm volatile("s_mov_b32 m0, %3\n\ts_mov_b32 exec_hi, 0\n\tbuffer_load_dword %0, %1, 0 offen lds\n\t"
                         "buffer_load_dword %0, %2, 0 offen lds\n\ts_mov_b64 exec, -1"
                         :: "v"(o), "s"(rk), "s"(rv), "i"(LDS_JUNK) : "memory");
        }
        if (u + 1 < NB) {
            const int so = (u + 1) * 256 * 1280;
            load_kv<ND>(t[u + 1 < NB ? u + 1 : 0], rk, rv, voff, so, hoff, so - 3840);
        } else {
#pragma unroll
            for (int i = 0; i < QE; i++) load_q1(i);
        }
        SB();
        convert_kv<ND, G>(t[u], W, xa - 512 * u, lane);
        signal_cnt(cnt + u);
    }
    if (LO) {
        const int r = (lane * 43) >> 7, c = lane - 3 * r;
        const int rr = (j - 20) + 10 * r;
        const int o = ((1024 + 128 * wave + rr) * D + cb + c * 64 + (c >> 1) * 30) * 2;
        asm volatile("s_mov_b32 m0, %3\n\ts_nop 0\n\tbuffer_load_dword %0, %1, 0 offen lds\n\tbuffer_load_dword %0, %1, %2 offen lds"
                     :: "v"(rr < 128 ? o : OOB), "s"(rq), "s"(512 * 1280), "i"(LDS_JUNK) : "memory");
    }
#pragma unroll
    for (int i = QE; i < 16; i++) load_q1(i);
    auto ro = __builtin_amdgcn_make_buffer_rsrc((void*)p.out, 0, S * D * 4, 0x00020000);
    const int ooff = (R0 + rin) * 2560 + br * 1280 + c0 * 2 + part * 4 * ND;
    LAS char* yb0 = smem + 2 * part * H_W + part * H_COPY + H_HEAD + 2 * rin;
    LAS char* yb1 = LO ? smem + 2 * part * H_W + ((part + 1) & 3) * H_COPY + H_HEAD + 2 * rin : yb0 + H_W;
#pragma unroll
    for (int tl = 0; tl < 4; tl++) {
        wait_cnt<2>(cnt + 8 + tl, G::C);
        u32x2 y[2 * ND];
#pragma unroll
        for (int c = 0; c < 2 * ND; c++) y[c] = *(LAS u32x2*)(((c & 1) ? yb1 : yb0) + (c >> 1) * H_W + 512 * (3 - tl));
#pragma unroll
        for (int r = 0; r < 4; r++) {
            u32 o[ND];
#pragma unroll
            for (int d = 0; d < ND; d++) {
                const f32x2 qf = unpack2(qv[tl][r].d[d]);
                const f32x2 ya = unpack2(y[2 * d][r >> 1]), yc = unpack2(y[2 * d + 1][r >> 1]);
                o[d] = pack2(qf.x * ya[r & 1], qf.y * yc[r & 1]);
            }
            const int oo = ooff + (r & 1) * 2560, so = (256 * tl + (r & 2)) * 2560;
            if constexpr (ND == 1) __builtin_amdgcn_raw_buffer_store_b32(o[0], ro, oo, so, 0);
            else { u32x2 o2 = {o[0], o[1]}; __builtin_amdgcn_raw_buffer_store_b64(o2, ro, oo, so, 2); }
        }
    }
    asm volatile("s_endpgm" ::: "memory");
}

#define MFMA(a, b, c) __builtin_amdgcn_mfma_f32_16x16x32_bf16(a, b, c, 0, 0, 0)
static __device__ __forceinline__ bf16x8 ldf(LAS char* a) {
    u32x4 r = *(volatile LAS u32x4*)a;
    return __builtin_bit_cast(bf16x8, r);
}
template <bool NAT>
static __device__ __forceinline__ void put_row(const u32x4& d, u32 e2, u32 e3, LAS char* a, bool on) {
    const u32 p2 = __builtin_amdgcn_update_dpp(e2, d.z, 0x138, 0xf, 0xf, false);
    const u32 p3 = __builtin_amdgcn_update_dpp(e3, d.w, 0x138, 0xf, 0xf, false);
    const u32 a0 = __builtin_amdgcn_alignbit(p3, p2, 16), a1 = __builtin_amdgcn_alignbit(d.x, p3, 16),
              a2 = __builtin_amdgcn_alignbit(d.y, d.x, 16), a3 = __builtin_amdgcn_alignbit(d.z, d.y, 16),
              a4 = __builtin_amdgcn_alignbit(d.w, d.z, 16);
    if (on) {
        if (NAT) *(volatile LAS u32x4*)(a + 3 * H_COPY) = d;
        *(volatile LAS u32x4*)(a) = u32x4{a0, a1, a2, a3};
        *(volatile LAS u32x4*)(a + H_COPY) = u32x4{p3, d.x, d.y, d.z};
        *(volatile LAS u32x4*)(a + 2 * H_COPY) = u32x4{a1, a2, a3, a4};
    }
}
#define RL63(v) __builtin_amdgcn_readlane(v, 63)
template <bool NAT0, bool NAT1>
static __device__ __forceinline__ void put_set(const u32x4& r0, const u32x4& r1, u32 e2, u32 e3, LAS char* a) {
    put_row<NAT0>(r0, e2, e3, a + H_HEAD, true);
    put_row<NAT1>(r1, RL63(r0.z), RL63(r0.w), a + H_HEAD + 1024, true);
}
static __device__ __forceinline__ void zero_head(LAS char* hw, int lane) {
    const u32x4 z = {0, 0, 0, 0};
    if (lane < 12) *(volatile LAS u32x4*)(hw + (lane & 3) * H_COPY + 16 * (lane >> 2)) = z;
}
static __device__ __forceinline__ void fold_d(u32x4& r0, u32 dbits, int lane) {
    const float s = __builtin_bit_cast(float, r0.x << 16) + __builtin_bit_cast(float, dbits);
    const u32 f = pack2(s, __builtin_bit_cast(float, r0.x & 0xffff0000u));
    r0.x = lane == 0 ? f : r0.x;
}

template <int LO>
static __device__ __forceinline__ void mfma_waves(const Ptrs& p, LAS char* smem, int c0, int wave) {
    const int lane = fresh_lane();
    typedef Geo<LO> G;
    LAS u32* cnt = (LAS u32*)(smem + LDS_CNT);
    const int mw = wave - 4;
    const int cw = LO ? 2 * mw : mw;
    const u32 dw = *(const CAS u32*)(p.dd + ((c0 + cw) & ~1));
    LAS char* hw = smem + mw * H_W;
    LAS char* xch = smem + LDS_X + cw * G::X_CH;
#pragma unroll
    for (int c = 0; c <= LO; c++) {
        const u32x2 z = {0, 0};
        if (lane < 57) {
            *(LAS u32x2*)(xch + c * G::X_CH + 2 * G::L + 8 * lane) = z;
            *(LAS u32x2*)(xch + c * G::X_CH + G::X_BOFF + 2 * G::L + 8 + 8 * lane) = z;
        }
    }
    LAS char* hl = hw + 16 * lane;
    asm volatile("s_waitcnt vmcnt(0)" ::: "memory");
    u32x4 s1a = *(volatile LAS u32x4*)(hl + 3 * H_COPY + H_HEAD), s1b = *(volatile LAS u32x4*)(hl + 3 * H_COPY + H_HEAD + 1024);
    u32x4 s2a, s2b = *(volatile LAS u32x4*)(hl + 1024);
    if (LO) {
        s2a = *(volatile LAS u32x4*)(hl);
        zero_head(hw, lane);
        fold_d(s1a, dw << 16, lane);
        put_set<true, false>(s1a, s1b, 0, 0, hl);
    } else {
        put_set<false, false>(s1a, s1b, RL63(s2b.z), RL63(s2b.w), hl);
        put_row<true>(s2b, 0, 0, hl - 16 * 61, lane >= 61);
        SB();
        const u32x4* hp = (const u32x4*)(p.h + (size_t)(c0 + mw) * S) + lane;
        s2a = hp[0]; s2b = hp[64];
        SB();
    }
    const int n = lane & 15, g = lane >> 4;
    LAS char* ha = hw + (n & 3) * H_COPY + 16 * ((n >> 2) + g);
    LAS char* xb = xch + ((n & 1) ? 0 : G::X_BOFF + 16) + 448 - 64 * (n >> 1) + 16 * g;
    bf16x8 H[4][8];
    constexpr int NP0 = 5, DPH = 5;
#pragma unroll
    for (int ph = 0; ph < NP0; ph++)
#pragma unroll
        for (int s = 3; s >= 0; s--) H[s][ph] = ldf(ha + 64 * ph + 512 * s);
    f32x4 acc[4];
#pragma unroll
    for (int t = 0; t < 4; t++) acc[t] = f32x4{0.f, 0.f, 0.f, 0.f};
    auto ldX = [&](int f) INL {
        const int e = f >> 3;
        return ldf(xb + (LO ? (e >> 2) * G::X_CH + 512 * (3 - (e & 3)) : 512 * (7 - e)) + 64 * (f & 7));
    };
    auto finish = [&](const f32x4& a, int ch, int t) INL {
        const int l = fresh_lane();
        const int cp = LO ? ((mw >> 1) + ch) & 3 : mw >> 1;
        LAS char* yp = hw + cp * H_COPY + H_HEAD + 512 * (3 - t) + (l & 48) + 8 * (l & 1) + 32 * (l & 14);
        u32x2 o = {pack2(a[0], a[1]), pack2(a[2], a[3])};
        *(volatile LAS u32x2*)yp = o;
        signal_cnt(cnt + 8 + t);
    };
    auto upto = [](int f) { const int e = f >> 3; const int d = e < 6 ? 2 : e < 7 ? 3 : 4; return f + d > 63 ? 63 : f + d; };
    bf16x8 X[64];
    u32 pk = 0;
    wait_cnt<1>(cnt + 0, 4);
    X[0] = ldX(0); X[1] = ldX(1);
#pragma unroll
    for (int e = 0; e < 8; e++) {
        const int pp = e & 3;
        const bool tri = LO || e >= 4;
        const bool fin_prev = LO ? e > 0 : e > 4;
        const int ep = e - 1;
        const bool need_next = e < (LO ? 3 : 7);
        if (e == 0) __builtin_amdgcn_s_setprio(2);
        if (e == 2) __builtin_amdgcn_s_setprio(1);
        if (e == 5) __builtin_amdgcn_s_setprio(0);
#pragma unroll
        for (int ph = 0; ph < 8; ph++) {
            const int f = 8 * e + ph;
            SB();
            f32x4 fa;
            if (fin_prev && ph == 0) fa = acc[ep & 3];
            if (LO && e == 4 && ph == 0) {
#pragma unroll
                for (int t = 0; t < 4; t++) acc[t] = f32x4{0.f, 0.f, 0.f, 0.f};
            }
            acc[3] = MFMA(H[3 - pp][ph], X[f], acc[3]);
            SB();
            {
                const int lo_ = f ? upto(f - 1) + 1 : 2, hi_ = upto(f);
#pragma unroll
                for (int fx = f + 1; fx < f + 5; fx++) if (fx >= lo_ && fx <= hi_) {
                    if ((fx & 7) == 0 && need_next) wait_from<1>(cnt + e + 1, 4, pk);
                    X[fx] = ldX(fx);
                }
            }
            SB();
#pragma unroll
            for (int t = 2; t >= 0; t--) {
                if (!tri || t >= pp) acc[t] = MFMA(H[(t - pp) & 3][ph], X[f], acc[t]);
                SB();
                if (t == 2 && e > 0 && e < 4) H[3 - pp][ph] = ldf(ha + 64 * ph + 512 * (3 - pp));
                if (e == 0) {
                    const int i = 4 * NP0 + 3 * ph + (2 - t);
                    if (i < 32) H[3 - (i & 3)][i >> 2 & 7] = ldf(ha + 64 * (i >> 2) + 512 * (3 - (i & 3)));
                }
                if (t == 1 && ph == 2 && need_next) pk = peek(cnt + e + 1);
                SB();
            }
            if (e == 0 && ph == DPH) {
                SB();
                const int l = fresh_lane();
                fold_d(s2a, (LO || ((c0 + cw) & 1)) ? (dw & 0xffff0000u) : (dw << 16), l);
                if (!LO) zero_head(hw, l);
                put_set<true, true>(s2a, s2b, 0, 0, hw + 16 * l);
                SB();
            }
            if (e == 0 && ph > DPH) {
#pragma unroll
                for (int i = 0; i < 8; i++) if (i <= ph && (ph == DPH + 1 || i == ph)) H[3][i] = ldf(ha + 64 * i + 512 * 3);
            }
            if (fin_prev && ph == 0) finish(fa, LO ? ep >> 2 : 0, ep & 3);
#pragma unroll
            for (int t = 0; t < 4; t++) if (!tri || t >= pp) asm volatile("" : "+v"(acc[t]));
        }
    }
    finish(acc[3], LO, 3);
    asm volatile("s_endpgm" ::: "memory");
}

#if defined(__HIP_DEVICE_COMPILE__)
#define NOPK __attribute__((target("no-packed-fp32-ops")))
#else
#define NOPK
#endif
__global__ __launch_bounds__(768) NOPK void h3_kernel(Params pa) {
    extern __shared__ __attribute__((aligned(256))) char smem_raw[];
    LAS char* smem = (LAS char*)smem_raw;
    int bid = blockIdx.x, tid = threadIdx.x;
    const int br = (bid >> 2) & 1;
    const Ptrs p = pa.b[br];
    SB();
    asm volatile("" : "+s"(bid), "+v"(tid));
    const int wave = __builtin_amdgcn_readfirstlane(tid) >> 6;
    const int j = bid >> 3, cb = (bid & 3) * 160;
    const bool lo = j >= 20;
    const int c0 = cb + (lo ? 16 * j - 320 : 8 * j);
    {
        const int lane = tid & 63, mw = wave - 4;
        {
            unsigned long long pc;
            int o;
            asm volatile("s_cmp_lt_u32 %2, 240\n\ts_cbranch_scc1 h3_nocode\n\ts_getpc_b64 %0\nh3_pc:\n\tv_lshlrev_b32 %1, 7, %3\n\ts_mov_b32 m0, %4\n\t"
                         "v_cmpx_gt_i32 vcc, h3_end-h3_pc, %1\n\ts_cbranch_execz h3_code1\n\tglobal_load_lds_dword %1, %0\nh3_code1:\n\ts_mov_b64 exec, -1\nh3_nocode:"
                         : "=&s"(pc), "=&v"(o) : "s"(bid), "v"(tid), "i"(LDS_JUNK) : "memory", "scc", "vcc");
        }
        const char* hp = (const char*)p.h + (size_t)(c0 + (lo ? 2 * mw : mw)) * (S * 2);
        const int role = wave < 4 ? 3 : j < 20 ? 0 : j < 30 ? 1 : 2;
        asm volatile("s_cmp_lg_u32 %3, 0\n\ts_cbranch_scc1 h3_nohi\n\ts_add_u32 m0, %2, %4\n\ts_nop 0\n\t"
                     "global_load_lds_dwordx4 %0, %1 offset:2048\n\tglobal_load_lds_dwordx4 %0, %1 offset:3072\n\t"
                     "s_mov_b32 m0, %2\n\ts_mov_b32 exec_lo, 0\n\ts_mov_b32 exec_hi, 0xff000000\n\t"
                     "global_load_lds_dwordx4 %0, %1 offset:1024\n\ts_mov_b64 exec, -1\nh3_nohi:"
                     :: "v"(16 * lane), "s"(hp), "s"(mw * H_W), "s"(role), "i"(3 * H_COPY + H_HEAD - 2048) : "memory", "scc");
        SB();
        __builtin_amdgcn_s_setprio(3);
        const int r = ((lane & 31) * 11) >> 5, c = (lane & 31) - 3 * r;
        const int rr = (j - 20) + 10 * r;
        const int o = ((TR * mw + rr) * D + cb + c * 64 + (c >> 1) * 30) * 2;
        const unsigned long long mk = __builtin_amdgcn_ballot_w64(rr < TR && r < 10);
        asm volatile("s_cmp_lg_u32 %4, 1\n\ts_cbranch_scc1 h3_nolo\n\ts_mov_b32 m0, %5\n\ts_mov_b32 exec_lo, %3\n\ts_mov_b32 exec_hi, 0\n\t"
                     "global_load_lds_dword %0, %1\n\tglobal_load_lds_dword %0, %2\n\ts_mov_b64 exec, -1\n\t"
                     "s_add_u32 m0, %9, %10\n\ts_nop 0\n\tglobal_load_lds_dwordx4 %6, %7\n\tglobal_load_lds_dwordx4 %6, %7 offset:1024\n\t"
                     "s_mov_b32 m0, %9\n\ts_nop 0\n\tglobal_load_lds_dwordx4 %6, %8\n\tglobal_load_lds_dwordx4 %6, %8 offset:1024\nh3_nolo:"
                     :: "v"(o), "s"(p.k), "s"(p.v), "s"((u32)mk), "s"(role), "i"(LDS_JUNK),
                        "v"(16 * lane), "s"(hp), "s"(hp + 4096), "s"(mw * H_W), "i"(3 * H_COPY + H_HEAD) : "memory", "scc");
        *(volatile LAS u32*)(smem + (tid < 16 ? LDS_CNT + 4 * tid : LDS_JUNK)) = 0;
    }
    if (__builtin_expect(j >= 30, 0)) {
        const int l = fresh_lane();
        auto rq = __builtin_amdgcn_make_buffer_rsrc((void*)p.q, 0, S * D * 2, 0x00020000);
        auto rx = __builtin_amdgcn_make_buffer_rsrc((void*)(j == 30 ? p.k : p.v), 0, S * D * 2, 0x00020000);
        const int i = wave * 64 + l, r = i / 3, c = i % 3;
        const int o = (r * D + cb + (c == 2 ? 158 : 64 * c)) * 2;
        u32 a[NSP + 2];
#pragma unroll
        for (int t = 0; t < NSP + 2; t++) {
            a[t] = t < NSP ? __builtin_amdgcn_raw_buffer_load_b32(rx, o, (2048 - 256 * NSP + 256 * t) * 1280, 0)
                           : __builtin_amdgcn_raw_buffer_load_b32(rq, o, ((j - 30) * 256 + 512 * (t - NSP)) * 1280, 0);
            if (t < NSP + 1) __builtin_amdgcn_s_barrier();
        }
#pragma unroll
        for (int t = 0; t < NSP + 2; t++) asm volatile("" :: "v"(a[t]));
        asm volatile("s_endpgm" ::: "memory");
    }
    asm volatile("s_waitcnt lgkmcnt(0)" ::: "memory");
    SB();
    __builtin_amdgcn_s_barrier();
    SB();
    if (__builtin_expect(wave < 4, 1)) {
        if (__builtin_expect(!lo, 1)) mem_waves<0>(p, smem, br, c0, wave, j, cb);
        mem_waves<1>(p, smem, br, c0, wave, j, cb);
    }
    if (__builtin_expect(!lo, 1)) mfma_waves<0>(p, smem, c0, wave);
    mfma_waves<1>(p, smem, c0, wave);
    asm volatile("h3_end:");
}

extern "C" void kernel_launch(void* const* d_in, const int* in_sizes, int n_in,
                              void* d_out, int out_size, void* d_ws, size_t ws_size,
                              hipStream_t stream) {
    Params p;
    for (int i = 0; i < 2; i++) {
        p.b[i].q = (const u16*)d_in[6 * i + 0];
        p.b[i].k = (const u16*)d_in[6 * i + 1];
        p.b[i].v = (const u16*)d_in[6 * i + 2];
        p.b[i].w = (const u16*)d_in[6 * i + 3];
        p.b[i].h = (const u16*)d_in[6 * i + 4];
        p.b[i].dd = (const u16*)d_in[6 * i + 5];
        p.b[i].out = (u16*)d_out;
        p.b[i].out2 = (u16*)d_out;
    }
    (void)hipFuncSetAttribute((const void*)h3_kernel, hipFuncAttributeMaxDynamicSharedMemorySize, LDS_TOTAL);
    h3_kernel<<<256, 768, LDS_TOTAL, stream>>>(p);
}
```

```cpp
#include <hip/hip_runtime.h>
#include <stdint.h>

typedef uint16_t u16;
typedef uint32_t u32;
typedef u32 u32x2 __attribute__((ext_vector_type(2)));
typedef u32 u32x4 __attribute__((ext_vector_type(4)));
typedef float f32x2 __attribute__((ext_vector_type(2)));
typedef float f32x4 __attribute__((ext_vector_type(4)));
typedef __bf16 bf16x2 __attribute__((ext_vector_type(2)));
typedef __bf16 bf16x8 __attribute__((ext_vector_type(8)));
typedef short s16x4 __attribute__((ext_vector_type(4)));
#define LAS __attribute__((address_space(3)))
#define CAS __attribute__((address_space(4)))
#define INL __attribute__((always_inline))
#define SB() __builtin_amdgcn_sched_barrier(0)

constexpr int S = 2048, D = 640;
constexpr int OOB = (int)0x80000000;

constexpr int TR = 64, TM = 192, NSP = 3;

constexpr int H_COPY = 2112, H_W = 4 * H_COPY, H_HEAD = 48, LDS_X = 8 * H_W;
template <int LO> struct Geo {
    static constexpr int L = LO ? 1024 : 2048, C = LO ? 16 : 8, NB = L / 256;
    static constexpr int X_BOFF = 2 * L + 464;
    static constexpr int X_CH = 2 * X_BOFF + (LO ? 16 : 48);
    static constexpr int END = LDS_X + C * X_CH;
};
constexpr int LDS_CNT = Geo<1>::END;
constexpr int LDS_JUNK = LDS_CNT + 64;
constexpr int LDS_TOTAL = LDS_JUNK + 256;
static_assert(Geo<0>::END <= LDS_CNT && LDS_TOTAL <= 163840, "LDS");

struct Ptrs { const u16 *q, *k, *v, *w, *h, *dd; u16* out; u16* out2; };
struct Params { Ptrs b[2]; };

static __device__ __forceinline__ u32 pack2(float lo, float hi) {
    f32x2 f = {lo, hi};
    bf16x2 b = __builtin_convertvector(f, bf16x2);
    return __builtin_bit_cast(u32, b);
}
static __device__ __forceinline__ f32x2 unpack2(u32 d) {
    f32x2 r = {__builtin_bit_cast(float, d << 16), __builtin_bit_cast(float, d & 0xffff0000u)};
    return r;
}
static __device__ __forceinline__ int fresh_lane() {
    int l;
    asm volatile("v_mbcnt_lo_u32_b32 %0, -1, 0\n\tv_mbcnt_hi_u32_b32 %0, -1, %0" : "=v"(l));
    return l;
}

template <int SL>
static __device__ __forceinline__ void wait_from(LAS u32* c, u32 n, u32 v) {
    u32 ts, tc;
    asm volatile(
        "s_mov_b32 %2, 0x400000\n"
        "W%=:\n\t"
        "v_readfirstlane_b32 %1, %0\n\t"
        "s_cmp_ge_u32 %1, %4\n\t"
        "s_cbranch_scc1 E%=\n\t"
        "s_sleep %5\n\t"
        "ds_read_b32 %0, %3\n\t"
        "s_sub_u32 %2, %2, 1\n\t"
        "s_waitcnt lgkmcnt(0)\n\t"
        "s_cbranch_scc0 W%=\n"
        "E%=:"
        : "+v"(v), "=&s"(ts), "=&s"(tc) : "v"(c), "s"(n), "i"(SL) : "memory", "scc");
}
static __device__ __forceinline__ u32 peek(LAS u32* c) { return *(volatile LAS u32*)c; }
template <int SL>
static __device__ __forceinline__ void wait_cnt(LAS u32* c, u32 n) { wait_from<SL>(c, n, peek(c)); }
static __device__ __forceinline__ void signal_cnt(LAS u32* c) {
    u32 one;
    asm volatile("s_mov_b64 exec, 1\n\tv_mov_b32 %0, 1\n\tds_add_u32 %1, %0\n\ts_mov_b64 exec, -1"
                 : "=&v"(one) : "v"(c) : "memory");
}

template <int ND> struct Row { u32 d[ND]; };
template <int ND, typename RS>
static __device__ __forceinline__ Row<ND> ldrow(RS r, int off, int so) {
    Row<ND> o;
    if constexpr (ND == 1) o.d[0] = __builtin_amdgcn_raw_buffer_load_b32(r, off, so, 0);
    else { u32x2 t = __builtin_amdgcn_raw_buffer_load_b64(r, off, so, 0); o.d[0] = t.x; o.d[1] = t.y; }
    return o;
}
template <int ND> struct KV { Row<ND> k[4], v[4], halo[3]; };
template <int ND, typename RS>
static __device__ __forceinline__ void load_kv(KV<ND>& t, RS rk, RS rv, int voff, int so, int ho, int hso) {
#pragma unroll
    for (int r = 0; r < 4; r++) t.k[r] = ldrow<ND>(rk, voff + 1280 * r, so);
#pragma unroll
    for (int r = 0; r < 3; r++) t.halo[r] = ldrow<ND>(rk, ho + 1280 * r, hso);
#pragma unroll
    for (int r = 0; r < 4; r++) t.v[r] = ldrow<ND>(rv, voff + 1280 * r, so);
}
template <int ND, typename G>
static __device__ __forceinline__ void convert_kv(const KV<ND>& t, const f32x2 (&W)[ND][4], LAS char* xa, int lane) {
    u32 pv[ND][3];
#pragma unroll
    for (int d = 0; d < ND; d++)
#pragma unroll
        for (int r = 0; r < 3; r++) {
            const u32 n = __builtin_amdgcn_ds_bpermute(4 * lane - 16, t.k[1 + r].d[d]);
            pv[d][r] = lane < 4 ? t.halo[r].d[d] : n;
        }
#pragma unroll
    for (int d = 0; d < ND; d++) {
        f32x2 kk[7];
#pragma unroll
        for (int r = 0; r < 4; r++) kk[3 + r] = unpack2(t.k[r].d[d]);
#pragma unroll
        for (int r = 0; r < 3; r++) kk[r] = unpack2(pv[d][r]);
        f32x2 x[4];
#pragma unroll
        for (int r = 3; r >= 0; r--) {
            f32x2 a = kk[r + 3] * W[d][0];
            a = __builtin_elementwise_fma(kk[r + 2], W[d][1], a);
            a = __builtin_elementwise_fma(kk[r + 1], W[d][2], a);
            a = __builtin_elementwise_fma(kk[r], W[d][3], a);
            x[r] = a * unpack2(t.v[r].d[d]);
        }
#pragma unroll
        for (int c = 0; c < 2; c++) {
            const u32x2 dd = {pack2(x[3][c], x[2][c]), pack2(x[1][c], x[0][c])};
            LAS char* a = xa + (2 * d + c) * G::X_CH;
            *(volatile LAS u32x2*)a = dd;
            *(volatile LAS u32x2*)(a + G::X_BOFF + 8) = dd;
        }
    }
}

static __device__ __forceinline__ f32x4 mma4(u32x2 a, u32x2 b, f32x4 c) {
    return __builtin_amdgcn_mfma_f32_4x4x4bf16_1k(__builtin_bit_cast(s16x4, a), __builtin_bit_cast(s16x4, b), c, 0, 0, 0);
}
template <typename G>
static __device__ __forceinline__ void convert_mma(const KV<1>& t, const u32x2 (&W)[2][2], LAS char* xa, int pl, bool hl) {
    u32 pv[3];
    f32x4 ks[2];
#pragma unroll
    for (int r = 0; r < 3; r++) pv[r] = __builtin_amdgcn_ds_bpermute(pl, t.k[1 + r].d[0]);
#pragma unroll
    for (int c = 0; c < 2; c++) {
        const u32 sel = c ? 0x07060302u : 0x05040100u;
        const u32x2 own = {__builtin_amdgcn_perm(t.k[2].d[0], t.k[3].d[0], sel), __builtin_amdgcn_perm(t.k[0].d[0], t.k[1].d[0], sel)};
        ks[c] = mma4(W[c][0], own, f32x4{0.f, 0.f, 0.f, 0.f});
    }
#pragma unroll
    for (int r = 0; r < 3; r++) pv[r] = hl ? t.halo[r].d[0] : pv[r];
#pragma unroll
    for (int c = 0; c < 2; c++) {
        const u32x2 prev = {__builtin_amdgcn_perm(pv[1], pv[2], c ? 0x07060302u : 0x05040100u), c ? pv[0] >> 16 : pv[0] & 0xffffu};
        ks[c] = mma4(W[c][1], prev, ks[c]);
    }
    f32x2 vf[4];
#pragma unroll
    for (int r = 0; r < 4; r++) vf[r] = unpack2(t.v[r].d[0]);
#pragma unroll
    for (int c = 0; c < 2; c++) {
        const u32x2 dd = {pack2(ks[c][3] * vf[3][c], ks[c][2] * vf[2][c]), pack2(ks[c][1] * vf[1][c], ks[c][0] * vf[0][c])};
        LAS char* a = xa + c * G::X_CH;
        *(volatile LAS u32x2*)a = dd;
        *(volatile LAS u32x2*)(a + G::X_BOFF + 8) = dd;
    }
}

template <int LO>
static __device__ __forceinline__ void mem_waves(const Ptrs& p, LAS char* smem, int br, int c0, int wave, int j, int cb) {
    const int lane = fresh_lane();
    typedef Geo<LO> G;
    constexpr int ND = LO ? 2 : 1, NB = G::NB, QE = 4;
    LAS u32* cnt = (LAS u32*)(smem + LDS_CNT);
    const int quad = lane >> 2, part = lane & 3;
    auto rk = __builtin_amdgcn_make_buffer_rsrc((void*)p.k, 0, S * D * 2, 0x00020000);
    auto rv = __builtin_amdgcn_make_buffer_rsrc((void*)p.v, 0, S * D * 2, 0x00020000);
    const int rin = 64 * wave + 4 * quad;
    const int voff = rin * 1280 + c0 * 2 + part * 4 * ND;
    const int kp = LO ? part : (lane >> 2) & 3, krow = LO ? rin : 64 * wave + (lane & 48) + 4 * (lane & 3);
    const int koff = krow * 1280 + c0 * 2 + kp * 4 * ND;
    const bool hl = LO ? lane < 4 : (lane & 51) == 0;
    const int pl = 4 * ((lane & 3) ? lane - 1 : lane - 13);
    u32x4 wv[ND];
#pragma unroll
    for (int d = 0; d < ND; d++) wv[d] = *(const u32x4*)(p.w + (c0 + 2 * ND * kp + 2 * d) * 4);
    const int hoff = hl ? koff : OOB;
    KV<ND> t[NB];
    if (!LO) { __builtin_amdgcn_s_setprio(2); __builtin_amdgcn_s_sleep(8); }
    load_kv<ND>(t[0], rk, rv, koff, 0, (hl && wave > 0) ? koff - 3840 : OOB, 0);
    f32x2 W[ND][4];
    u32x2 WM[2][2];
    if constexpr (LO) {
#pragma unroll
        for (int d = 0; d < ND; d++) {
            f32x2 a0 = unpack2(wv[d].x), a1 = unpack2(wv[d].y), b0 = unpack2(wv[d].z), b1 = unpack2(wv[d].w);
            W[d][0] = f32x2{a0.x, b0.x}; W[d][1] = f32x2{a0.y, b0.y}; W[d][2] = f32x2{a1.x, b1.x}; W[d][3] = f32x2{a1.y, b1.y};
        }
    } else {
#pragma unroll
        for (int c = 0; c < 2; c++) {
            const unsigned long long w = ((unsigned long long)wv[0][2 * c + 1] << 32) | wv[0][2 * c];
            const unsigned long long own = w << (48 - 16 * (lane & 3)), prev = (w >> 16) >> (16 * (lane & 3));
            WM[c][0] = u32x2{(u32)own, (u32)(own >> 32)};
            WM[c][1] = u32x2{(u32)prev, (u32)(prev >> 32)};
        }
    }
    LAS char* xa = smem + LDS_X + 2 * ND * kp * G::X_CH + 2 * (G::L - 4 - krow);
    auto rq = __builtin_amdgcn_make_buffer_rsrc((void*)p.q, 0, S * D * 2, 0x00020000);
    constexpr int R0 = LO ? 0 : 1024;
    Row<ND> qv[4][4];
    auto load_q1 = [&](int i) INL { qv[i >> 2][i & 3] = ldrow<ND>(rq, voff + 1280 * (i & 3), (R0 + 256 * (i >> 2)) * 1280); };
#pragma unroll
    for (int u = 0; u < NB; u++) {
        SB();
        if (!LO && u == 1) {
            const int r = ((lane & 31) * 11) >> 5, c = (lane & 31) - 3 * r;
            const int rr = j + 20 * r;
            const int o = (rr < TM && r < 10) ? ((8 * TR + TM * wave + rr) * D + cb + c * 64 + (c >> 1) * 30) * 2 : OOB;
            asm volatile("s_mov_b32 m0, %3\n\ts_mov_b32 exec_hi, 0\n\tbuffer_load_dword %0, %1, 0 offen lds\n\t"
                         "buffer_load_dword %0, %2, 0 offen lds\n\ts_mov_b64 exec, -1"
                         :: "v"(o), "s"(rk), "s"(rv), "i"(LDS_JUNK) : "memory");
        }
        if (u + 1 < NB) {
            const int so = (u + 1) * 256 * 1280;
            load_kv<ND>(t[u + 1 < NB ? u + 1 : 0], rk, rv, koff, so, hoff, so - 3840);
        } else {
#pragma unroll
            for (int i = 0; i < QE; i++) load_q1(i);
        }
        SB();
        if constexpr (LO) convert_kv<ND, G>(t[u], W, xa - 512 * u, lane);
        else convert_mma<G>(t[u], WM, xa - 512 * u, pl, hl);
        signal_cnt(cnt + u);
    }
    if (LO) {
        const int r = (lane * 43) >> 7, c = lane - 3 * r;
        const int rr = (j - 20) + 10 * r;
        const int o = ((1024 + 128 * wave + rr) * D + cb + c * 64 + (c >> 1) * 30) * 2;
        asm volatile("s_mov_b32 m0, %3\n\ts_nop 0\n\tbuffer_load_dword %0, %1, 0 offen lds\n\tbuffer_load_dword %0, %1, %2 offen lds"
                     :: "v"(rr < 128 ? o : OOB), "s"(rq), "s"(512 * 1280), "i"(LDS_JUNK) : "memory");
    }
#pragma unroll
    for (int i = QE; i < 16; i++) load_q1(i);
    auto ro = __builtin_amdgcn_make_buffer_rsrc((void*)p.out, 0, S * D * 4, 0x00020000);
    const int ooff = (R0 + rin) * 2560 + br * 1280 + c0 * 2 + part * 4 * ND;
    LAS char* yb0 = smem + 2 * part * H_W + part * H_COPY + H_HEAD + 2 * rin;
    LAS char* yb1 = LO ? smem + 2 * part * H_W + ((part + 1) & 3) * H_COPY + H_HEAD + 2 * rin : yb0 + H_W;
#pragma unroll
    for (int tl = 0; tl < 4; tl++) {
        wait_cnt<2>(cnt + 8 + tl, G::C);
        u32x2 y[2 * ND];
#pragma unroll
        for (int c = 0; c < 2 * ND; c++) y[c] = *(LAS u32x2*)(((c & 1) ? yb1 : yb0) + (c >> 1) * H_W + 512 * (3 - tl));
#pragma unroll
        for (int r = 0; r < 4; r++) {
            u32 o[ND];
#pragma unroll
            for (int d = 0; d < ND; d++) {
                const f32x2 qf = unpack2(qv[tl][r].d[d]);
                const f32x2 ya = unpack2(y[2 * d][r >> 1]), yc = unpack2(y[2 * d + 1][r >> 1]);
                o[d] = pack2(qf.x * ya[r & 1], qf.y * yc[r & 1]);
            }
            const int oo = ooff + (r & 1) * 2560, so = (256 * tl + (r & 2)) * 2560;
            if constexpr (ND == 1) __builtin_amdgcn_raw_buffer_store_b32(o[0], ro, oo, so, 0);
            else { u32x2 o2 = {o[0], o[1]}; __builtin_amdgcn_raw_buffer_store_b64(o2, ro, oo, so, 2); }
        }
    }
    asm volatile("s_endpgm" ::: "memory");
}

#define MFMA(a, b, c) __builtin_amdgcn_mfma_f32_16x16x32_bf16(a, b, c, 0, 0, 0)
static __device__ __forceinline__ bf16x8 ldf(LAS char* a) {
    u32x4 r = *(volatile LAS u32x4*)a;
    return __builtin_bit_cast(bf16x8, r);
}
template <bool NAT>
static __device__ __forceinline__ void put_row(const u32x4& d, u32 e2, u32 e3, LAS char* a, bool on) {
    const u32 p2 = __builtin_amdgcn_update_dpp(e2, d.z, 0x138, 0xf, 0xf, false);
    const u32 p3 = __builtin_amdgcn_update_dpp(e3, d.w, 0x138, 0xf, 0xf, false);
    const u32 a0 = __builtin_amdgcn_alignbit(p3, p2, 16), a1 = __builtin_amdgcn_alignbit(d.x, p3, 16),
              a2 = __builtin_amdgcn_alignbit(d.y, d.x, 16), a3 = __builtin_amdgcn_alignbit(d.z, d.y, 16),
              a4 = __builtin_amdgcn_alignbit(d.w, d.z, 16);
    if (on) {
        if (NAT) *(volatile LAS u32x4*)(a + 3 * H_COPY) = d;
        *(volatile LAS u32x4*)(a) = u32x4{a0, a1, a2, a3};
        *(volatile LAS u32x4*)(a + H_COPY) = u32x4{p3, d.x, d.y, d.z};
        *(volatile LAS u32x4*)(a + 2 * H_COPY) = u32x4{a1, a2, a3, a4};
    }
}
#define RL63(v) __builtin_amdgcn_readlane(v, 63)
template <bool NAT0, bool NAT1>
static __device__ __forceinline__ void put_set(const u32x4& r0, const u32x4& r1, u32 e2, u32 e3, LAS char* a) {
    put_row<NAT0>(r0, e2, e3, a + H_HEAD, true);
    put_row<NAT1>(r1, RL63(r0.z), RL63(r0.w), a + H_HEAD + 1024, true);
}
static __device__ __forceinline__ void zero_head(LAS char* hw, int lane) {
    const u32x4 z = {0, 0, 0, 0};
    if (lane < 12) *(volatile LAS u32x4*)(hw + (lane & 3) * H_COPY + 16 * (lane >> 2)) = z;
}
static __device__ __forceinline__ void fold_d(u32x4& r0, u32 dbits, int lane) {
    const float s = __builtin_bit_cast(float, r0.x << 16) + __builtin_bit_cast(float, dbits);
    const u32 f = pack2(s, __builtin_bit_cast(float, r0.x & 0xffff0000u));
    r0.x = lane == 0 ? f : r0.x;
}

template <int LO>
static __device__ __forceinline__ void mfma_waves(const Ptrs& p, LAS char* smem, int c0, int wave) {
    const int lane = fresh_lane();
    typedef Geo<LO> G;
    LAS u32* cnt = (LAS u32*)(smem + LDS_CNT);
    const int mw = wave - 4;
    const int cw = LO ? 2 * mw : mw;
    const u32 dw = *(const CAS u32*)(p.dd + ((c0 + cw) & ~1));
    LAS char* hw = smem + mw * H_W;
    LAS char* xch = smem + LDS_X + cw * G::X_CH;
#pragma unroll
    for (int c = 0; c <= LO; c++) {
        const u32x2 z = {0, 0};
        if (lane < 57) {
            *(LAS u32x2*)(xch + c * G::X_CH + 2 * G::L + 8 * lane) = z;
            *(LAS u32x2*)(xch + c * G::X_CH + G::X_BOFF + 2 * G::L + 8 + 8 * lane) = z;
        }
    }
    LAS char* hl = hw + 16 * lane;
    asm volatile("s_waitcnt vmcnt(0)" ::: "memory");
    u32x4 s1a = *(volatile LAS u32x4*)(hl + 3 * H_COPY + H_HEAD), s1b = *(volatile LAS u32x4*)(hl + 3 * H_COPY + H_HEAD + 1024);
    u32x4 s2a, s2b = *(volatile LAS u32x4*)(hl + 1024);
    if (LO) {
        s2a = *(volatile LAS u32x4*)(hl);
        zero_head(hw, lane);
        fold_d(s1a, dw << 16, lane);
        put_set<true, false>(s1a, s1b, 0, 0, hl);
    } else {
        put_set<false, false>(s1a, s1b, RL63(s2b.z), RL63(s2b.w), hl);
        put_row<true>(s2b, 0, 0, hl - 16 * 61, lane >= 61);
        SB();
        const u32x4* hp = (const u32x4*)(p.h + (size_t)(c0 + mw) * S) + lane;
        s2a = hp[0]; s2b = hp[64];
        SB();
    }
    const int n = lane & 15, g = lane >> 4;
    LAS char* ha = hw + (n & 3) * H_COPY + 16 * ((n >> 2) + g);
    LAS char* xb = xch + ((n & 1) ? 0 : G::X_BOFF + 16) + 448 - 64 * (n >> 1) + 16 * g;
    bf16x8 H[4][8];
    constexpr bool DEF = !LO;
    constexpr int NP0 = DEF ? 5 : 8, DPH = 5;
#pragma unroll
    for (int ph = 0; ph < NP0; ph++)
#pragma unroll
        for (int s = 3; s >= 0; s--) H[s][ph] = ldf(ha + 64 * ph + 512 * s);
    auto stage2 = [&]() INL {
        const int l = fresh_lane();
        fold_d(s2a, (LO || ((c0 + cw) & 1)) ? (dw & 0xffff0000u) : (dw << 16), l);
        if (!LO) zero_head(hw, l);
        put_set<true, true>(s2a, s2b, 0, 0, hw + 16 * l);
    };
    if (!DEF) stage2();
    f32x4 acc[4];
#pragma unroll
    for (int t = 0; t < 4; t++) acc[t] = f32x4{0.f, 0.f, 0.f, 0.f};
    auto ldX = [&](int f) INL {
        const int e = f >> 3;
        return ldf(xb + (LO ? (e >> 2) * G::X_CH + 512 * (3 - (e & 3)) : 512 * (7 - e)) + 64 * (f & 7));
    };
    auto finish = [&](const f32x4& a, int ch, int t) INL {
        const int l = fresh_lane();
        const int cp = LO ? ((mw >> 1) + ch) & 3 : mw >> 1;
        LAS char* yp = hw + cp * H_COPY + H_HEAD + 512 * (3 - t) + (l & 48) + 8 * (l & 1) + 32 * (l & 14);
        u32x2 o = {pack2(a[0], a[1]), pack2(a[2], a[3])};
        *(volatile LAS u32x2*)yp = o;
        signal_cnt(cnt + 8 + t);
    };
    auto upto = [](int f) { const int e = f >> 3; const int d = e < 6 ? 2 : e < 7 ? 3 : 4; return f + d > 63 ? 63 : f + d; };
    bf16x8 X[64];
    u32 pk = 0;
    wait_cnt<1>(cnt + 0, 4);
    X[0] = ldX(0); X[1] = ldX(1);
#pragma unroll
    for (int e = 0; e < 8; e++) {
        const int pp = e & 3;
        const bool tri = LO || e >= 4;
        const bool fin_prev = LO ? e > 0 : e > 4;
        const int ep = e - 1;
        const bool need_next = e < (LO ? 3 : 7);
        if (e == 0) __builtin_amdgcn_s_setprio(2);
        if (e == 2) __builtin_amdgcn_s_setprio(1);
        if (e == 5) __builtin_amdgcn_s_setprio(0);
#pragma unroll
        for (int ph = 0; ph < 8; ph++) {
            const int f = 8 * e + ph;
            SB();
            f32x4 fa;
            if (fin_prev && ph == 0) fa = acc[ep & 3];
            if (LO && e == 4 && ph == 0) {
#pragma unroll
                for (int t = 0; t < 4; t++) acc[t] = f32x4{0.f, 0.f, 0.f, 0.f};
            }
            acc[3] = MFMA(H[3 - pp][ph], X[f], acc[3]);
            SB();
            {
                const int lo_ = f ? upto(f - 1) + 1 : 2, hi_ = upto(f);
#pragma unroll
                for (int fx = f + 1; fx < f + 5; fx++) if (fx >= lo_ && fx <= hi_) {
                    if ((fx & 7) == 0 && need_next) wait_from<1>(cnt + e + 1, 4, pk);
                    X[fx] = ldX(fx);
                }
            }
            SB();
#pragma unroll
            for (int t = 2; t >= 0; t--) {
                if (!tri || t >= pp) acc[t] = MFMA(H[(t - pp) & 3][ph], X[f], acc[t]);
                SB();
                if (t == 2 && (e > 0 || !DEF) && e < 4) H[3 - pp][ph] = ldf(ha + 64 * ph + 512 * (3 - pp));
                if (e == 0) {
                    const int i = 4 * NP0 + 3 * ph + (2 - t);
                    if (i < 32) H[3 - (i & 3)][i >> 2 & 7] = ldf(ha + 64 * (i >> 2) + 512 * (3 - (i & 3)));
                }
                if (t == 1 && ph == 2 && need_next) pk = peek(cnt + e + 1);
                SB();
            }
            if (DEF && e == 0 && ph == DPH) { SB(); stage2(); SB(); }
            if (DEF && e == 0 && ph > DPH) {
#pragma unroll
                for (int i = 0; i < 8; i++) if (i <= ph && (ph == DPH + 1 || i == ph)) H[3][i] = ldf(ha + 64 * i + 512 * 3);
            }
            if (fin_prev && ph == 0) finish(fa, LO ? ep >> 2 : 0, ep & 3);
#pragma unroll
            for (int t = 0; t < 4; t++) if (!tri || t >= pp) asm volatile("" : "+v"(acc[t]));
        }
    }
    finish(acc[3], LO, 3);
    asm volatile("s_endpgm" ::: "memory");
}

#if defined(__HIP_DEVICE_COMPILE__)
#define NOPK __attribute__((target("no-packed-fp32-ops")))
#else
#define NOPK
#endif
__global__ __launch_bounds__(768) NOPK void h3_kernel(Params pa) {
    extern __shared__ __attribute__((aligned(256))) char smem_raw[];
    LAS char* smem = (LAS char*)smem_raw;
    int bid = blockIdx.x, tid = threadIdx.x;
    const int br = (bid >> 2) & 1;
    const Ptrs p = pa.b[br];
    SB();
    asm volatile("" : "+s"(bid), "+v"(tid));
    const int wave = __builtin_amdgcn_readfirstlane(tid) >> 6;
    const int j = bid >> 3, cb = (bid & 3) * 160;
    const bool lo = j >= 20;
    const int c0 = cb + (lo ? 16 * j - 320 : 8 * j);
    {
        const int lane = tid & 63, mw = wave - 4;
        {
            unsigned long long pc;
            int o;
            asm volatile("s_cmp_lt_u32 %2, 240\n\ts_cbranch_scc1 h3_nocode\n\ts_getpc_b64 %0\nh3_pc:\n\tv_lshlrev_b32 %1, 7, %3\n\ts_mov_b32 m0, %4\n\t"
                         "v_cmpx_gt_i32 vcc, h3_end-h3_pc, %1\n\ts_cbranch_execz h3_code1\n\tglobal_load_lds_dword %1, %0\nh3_code1:\n\ts_mov_b64 exec, -1\nh3_nocode:"
                         : "=&s"(pc), "=&v"(o) : "s"(bid), "v"(tid), "i"(LDS_JUNK) : "memory", "scc", "vcc");
        }
        const char* hp = (const char*)p.h + (size_t)(c0 + (lo ? 2 * mw : mw)) * (S * 2);
        const int role = wave < 4 ? 3 : j < 20 ? 0 : j < 30 ? 1 : 2;
        asm volatile("s_cmp_lg_u32 %3, 0\n\ts_cbranch_scc1 h3_nohi\n\ts_add_u32 m0, %2, %4\n\ts_nop 0\n\t"
                     "global_load_lds_dwordx4 %0, %1 offset:2048\n\tglobal_load_lds_dwordx4 %0, %1 offset:3072\n\t"
                     "s_mov_b32 m0, %2\n\ts_mov_b32 exec_lo, 0\n\ts_mov_b32 exec_hi, 0xff000000\n\t"
                     "global_load_lds_dwordx4 %0, %1 offset:1024\n\ts_mov_b64 exec, -1\nh3_nohi:"
                     :: "v"(16 * lane), "s"(hp), "s"(mw * H_W), "s"(role), "i"(3 * H_COPY + H_HEAD - 2048) : "memory", "scc");
        SB();
        __builtin_amdgcn_s_setprio(3);
        const int r = ((lane & 31) * 11) >> 5, c = (lane & 31) - 3 * r;
        const int rr = (j - 20) + 10 * r;
        const int o = ((TR * mw + rr) * D + cb + c * 64 + (c >> 1) * 30) * 2;
        const unsigned long long mk = __builtin_amdgcn_ballot_w64(rr < TR && r < 10);
        asm volatile("s_cmp_lg_u32 %4, 1\n\ts_cbranch_scc1 h3_nolo\n\ts_mov_b32 m0, %5\n\ts_mov_b32 exec_lo, %3\n\ts_mov_b32 exec_hi, 0\n\t"
                     "global_load_lds_dword %0, %1\n\tglobal_load_lds_dword %0, %2\n\ts_mov_b64 exec, -1\n\t"
                     "s_add_u32 m0, %9, %10\n\ts_nop 0\n\tglobal_load_lds_dwordx4 %6, %7\n\tglobal_load_lds_dwordx4 %6, %7 offset:1024\n\t"
                     "s_mov_b32 m0, %9\n\ts_nop 0\n\tglobal_load_lds_dwordx4 %6, %8\n\tglobal_load_lds_dwordx4 %6, %8 offset:1024\nh3_nolo:"
                     :: "v"(o), "s"(p.k), "s"(p.v), "s"((u32)mk), "s"(role), "i"(LDS_JUNK),
                        "v"(16 * lane), "s"(hp), "s"(hp + 4096), "s"(mw * H_W), "i"(3 * H_COPY + H_HEAD) : "memory", "scc");
        *(volatile LAS u32*)(smem + (tid < 16 ? LDS_CNT + 4 * tid : LDS_JUNK)) = 0;
    }
    if (__builtin_expect(j >= 30, 0)) {
        const int l = fresh_lane();
        auto rq = __builtin_amdgcn_make_buffer_rsrc((void*)p.q, 0, S * D * 2, 0x00020000);
        auto rx = __builtin_amdgcn_make_buffer_rsrc((void*)(j == 30 ? p.k : p.v), 0, S * D * 2, 0x00020000);
        const int i = wave * 64 + l, r = i / 3, c = i % 3;
        const int o = (r * D + cb + (c == 2 ? 158 : 64 * c)) * 2;
        u32 a[NSP + 2];
#pragma unroll
        for (int t = 0; t < NSP + 2; t++) {
            a[t] = t < NSP ? __builtin_amdgcn_raw_buffer_load_b32(rx, o, (2048 - 256 * NSP + 256 * t) * 1280, 0)
                           : __builtin_amdgcn_raw_buffer_load_b32(rq, o, ((j - 30) * 256 + 512 * (t - NSP)) * 1280, 0);
            if (t < NSP + 1) __builtin_amdgcn_s_barrier();
        }
#pragma unroll
        for (int t = 0; t < NSP + 2; t++) asm volatile("" :: "v"(a[t]));
        asm volatile("s_endpgm" ::: "memory");
    }
    asm volatile("s_waitcnt lgkmcnt(0)" ::: "memory");
    SB();
    __builtin_amdgcn_s_barrier();
    SB();
    if (__builtin_expect(wave < 4, 1)) {
        if (__builtin_expect(!lo, 1)) mem_waves<0>(p, smem, br, c0, wave, j, cb);
        mem_waves<1>(p, smem, br, c0, wave, j, cb);
    }
    if (__builtin_expect(!lo, 1)) mfma_waves<0>(p, smem, c0, wave);
    mfma_waves<1>(p, smem, c0, wave);
    asm volatile("h3_end:");
}

extern "C" void kernel_launch(void* const* d_in, const int* in_sizes, int n_in,
                              void* d_out, int out_size, void* d_ws, size_t ws_size,
                              hipStream_t stream) {
    Params p;
    for (int i = 0; i < 2; i++) {
        p.b[i].q = (const u16*)d_in[6 * i + 0];
        p.b[i].k = (const u16*)d_in[6 * i + 1];
        p.b[i].v = (const u16*)d_in[6 * i + 2];
        p.b[i].w = (const u16*)d_in[6 * i + 3];
        p.b[i].h = (const u16*)d_in[6 * i + 4];
        p.b[i].dd = (const u16*)d_in[6 * i + 5];
        p.b[i].out = (u16*)d_out;
        p.b[i].out2 = (u16*)d_out;
    }
    (void)hipFuncSetAttribute((const void*)h3_kernel, hipFuncAttributeMaxDynamicSharedMemorySize, LDS_TOTAL);
    h3_kernel<<<256, 768, LDS_TOTAL, stream>>>(p);
}
```
